```python
import torch

def _softmax(x, axis=-1):
    return torch.softmax(x, dim=axis)

def _gelu(x):
    return torch.nn.functional.gelu(x, approximate='tanh')

def _silu(x):
    return torch.nn.functional.silu(x)

def _sigmoid(x):
    return torch.sigmoid(x)

def _layernorm(x, axis=-1, eps=1e-05):
    m = x.mean(dim=axis, keepdim=True)
    v = x.var(dim=axis, keepdim=True, unbiased=False)
    return (x - m) / torch.sqrt(v + eps)

def _scatter_add(tgt, idx, src, axis=0):
    return tgt.clone().index_add_(axis, idx, src)

def _segment_sum(src, seg, n):
    out = torch.zeros((n,) + src.shape[1:], dtype=src.dtype)
    return out.index_add_(0, seg, src)

def _pad(x, pw):
    flat = tuple((v for pair in reversed(pw) for v in pair))
    return torch.nn.functional.pad(x, flat)
kernel_name = 'adaLN_cond__gqa_block_cached__glu_gated_geglu__prompt'

def setup_inputs():

    _w = lambda a: (lambda t: t.to(torch.bfloat16) if t.is_floating_point() else t)(torch.as_tensor(a))

    def _A_setup():
        B, Sq, D = (1, 2048, 512)
        Hq, Hkv, Sk, Dh = (32, 32, 2048, 128)
        assert Hq % Hkv == 0, (Hq, Hkv)
        return {'x': _w((lambda *s: torch.randn(*s, dtype=torch.bfloat16) * 0.05)(B, Sq, D)), 'k_cache': _w((lambda *s: torch.randn(*s, dtype=torch.bfloat16) * 0.8)(B, Hkv, Sk, Dh)), 'v_cache': _w((lambda *s: torch.randn(*s, dtype=torch.bfloat16) * 3.2)(B, Hkv, Sk, Dh)), 'wq': _w((lambda *s: torch.randn(*s, dtype=torch.bfloat16) * 0.8)(D, Hq * Dh) * D ** (-0.5)), 'wo': _w((lambda *s: torch.randn(*s, dtype=torch.bfloat16) * 3.2)(Hq * Dh, D) * (Hq * Dh) ** (-0.5))}
    _A_in = _A_setup()

    def _B_setup():
        B, S, Dm, Df = (1, 2048, 512, 1376)
        return {'x': _w((lambda *s: torch.randn(*s, dtype=torch.bfloat16) * 0.05)(B, S, Dm)), 'w_gate': _w((lambda *s: torch.randn(*s, dtype=torch.bfloat16) * 3.2)(Dm, Df) * Dm ** (-0.5)), 'w_up': _w((lambda *s: torch.randn(*s, dtype=torch.bfloat16) * 3.2)(Dm, Df) * Dm ** (-0.5)), 'w_down': _w((lambda *s: torch.randn(*s, dtype=torch.bfloat16) * 3.2)(Df, Dm) * Df ** (-0.5))}
    _B_in = _B_setup()
    return {'A_x': _A_in['x'], 'A_k_cache': _A_in['k_cache'], 'A_v_cache': _A_in['v_cache'], 'A_wq': _A_in['wq'], 'A_wo': _A_in['wo'], 'B_x': _B_in['x'], 'B_w_gate': _B_in['w_gate'], 'B_w_up': _B_in['w_up'], 'B_w_down': _B_in['w_down'], 'c': _w((lambda *s: torch.randn(*s, dtype=torch.bfloat16) * 0.4)(1, 512) * 8.0), 'w_mod': _w((lambda *s: torch.randn(*s, dtype=torch.bfloat16) * 0.4)(512, 6 * 512) * 512 ** (-0.5) * 8.0)}

def reference(A_x, A_k_cache, A_v_cache, A_wq, A_wo, B_x, B_w_gate, B_w_up, B_w_down, c, w_mod):

    def _A_ref(x, k_cache, v_cache, wq, wo):
        B, Sq, D = x.shape
        Hq, Dh = (32, 128)
        Hkv = k_cache.shape[1]
        rep = Hq // Hkv
        q = (lambda x, *d: x.permute(*d))((x @ wq).reshape(B, Sq, Hq, Dh), 0, 2, 1, 3)
        k = (lambda x, n, axis: torch.repeat_interleave(x, n, dim=axis))(k_cache, rep, axis=1)
        v = (lambda x, n, axis: torch.repeat_interleave(x, n, dim=axis))(v_cache, rep, axis=1)
        scores = torch.einsum('bhqd,bhkd->bhqk', q, k) * Dh ** (-0.5)
        p = _softmax(scores, axis=-1)
        o = torch.einsum('bhqk,bhkd->bhqd', p, v)
        return (lambda x, *d: x.permute(*d))(o, 0, 2, 1, 3).reshape(B, Sq, Hq * Dh) @ wo

    def _B_ref(x, w_gate, w_up, w_down):
        g = torch.einsum('bsd,df->bsf', x, w_gate)
        u = torch.einsum('bsd,df->bsf', x, w_up)
        h = _gelu(g) * u
        return torch.einsum('bsf,fd->bsd', h, w_down)
    x = A_x
    mod = (_silu(c) @ w_mod).reshape(c.shape[0], 6, 1, -1)
    sa, ca, ga, sb, cb, gb = (mod[:, 0].clone(), mod[:, 1].clone(), mod[:, 2].clone(), mod[:, 3].clone(), mod[:, 4].clone(), mod[:, 5].clone())
    h = _layernorm(x, axis=-1) * (1 + ca) + sa
    x = x + ga * _A_ref(h, A_k_cache, A_v_cache, A_wq, A_wo)
    h = _layernorm(x, axis=-1) * (1 + cb) + sb
    x = x + gb * _B_ref(h, B_w_gate, B_w_up, B_w_down)
    return x
```

```cpp
#include <hip/hip_runtime.h>
#include <hip/hip_bf16.h>

using bf16 = __hip_bfloat16;
using bf16x8 = __attribute__((ext_vector_type(8))) short;
using s16x4  = __attribute__((ext_vector_type(4))) short;
using f32x16 = __attribute__((ext_vector_type(16))) float;
using f32x8  = __attribute__((ext_vector_type(8))) float;
using u32x4  = __attribute__((ext_vector_type(4))) unsigned;

constexpr int SEQ = 2048, DM = 512, NH = 32, DH = 128, DQ = 4096, DF = 1376;

__device__ __forceinline__ float bf2f(unsigned short u) { return __uint_as_float(((unsigned)u) << 16); }
__device__ __forceinline__ unsigned short f2bf(float f) { bf16 b = __float2bfloat16(f); return *reinterpret_cast<unsigned short*>(&b); }
__device__ __forceinline__ float rbf(float f) { return bf2f(f2bf(f)); }

__global__ __launch_bounds__(256) void k_mod(const unsigned short* __restrict__ c, const unsigned short* __restrict__ w, float* __restrict__ mod) {
  __shared__ float sc[512];
  __shared__ float red[4][64];
  int t = threadIdx.x;
  for (int i = t; i < 512; i += 256) { float v = bf2f(c[i]); float s = v / (1.f + __expf(-v)); sc[i] = rbf(s); }
  __syncthreads();
  int col = blockIdx.x * 64 + (t & 63), kp = t >> 6;
  float acc = 0.f;
  for (int k = kp * 128; k < kp * 128 + 128; ++k) acc += sc[k] * bf2f(w[(long)k * 3072 + col]);
  red[kp][t & 63] = acc;
  __syncthreads();
  if (t < 64) { float s = red[0][t] + red[1][t] + red[2][t] + red[3][t]; mod[blockIdx.x * 64 + t] = rbf(s); }
}

__global__ __launch_bounds__(256) void k_ln(const unsigned short* __restrict__ x, const float* __restrict__ sh, const float* __restrict__ sc, unsigned short* __restrict__ h) {
  int row = blockIdx.x * 4 + (threadIdx.x >> 6), lane = threadIdx.x & 63;
  const unsigned short* xr = x + (long)row * DM;
  float v[8];
  bf16x8 xv = *reinterpret_cast<const bf16x8*>(xr + lane * 8);
  float s = 0.f;
  for (int j = 0; j < 8; ++j) { v[j] = bf2f((unsigned short)xv[j]); s += v[j]; }
  for (int o = 32; o > 0; o >>= 1) s += __shfl_xor(s, o);
  float mean = s * (1.f / DM);
  float q = 0.f;
  for (int j = 0; j < 8; ++j) { float d = v[j] - mean; q += d * d; }
  for (int o = 32; o > 0; o >>= 1) q += __shfl_xor(q, o);
  float var = rbf(q * (1.f / DM));
  float mb = rbf(mean);
  float den = rbf(sqrtf(rbf(var + 1e-5f)));
  bf16x8 out;
  for (int j = 0; j < 8; ++j) {
    int k = lane * 8 + j;
    float xn = rbf(rbf(v[j] - mb) / den);
    float c1 = rbf(1.f + sc[k]);
    float t = rbf(rbf(xn * c1) + sh[k]);
    out[j] = (short)f2bf(t);
  }
  *reinterpret_cast<bf16x8*>(h + (long)row * DM + lane * 8) = out;
}

template <int EPI>
__global__ __launch_bounds__(256) void k_gemm(const unsigned short* __restrict__ A, const unsigned short* __restrict__ B, unsigned short* __restrict__ C,
                                              int M, int N, int K, const unsigned short* __restrict__ res, const float* __restrict__ g) {
  __shared__ __attribute__((aligned(16))) unsigned short sA[64][40];
  __shared__ __attribute__((aligned(16))) unsigned short sB[64][40];
  int t = threadIdx.x, wid = t >> 6, lane = t & 63, r32 = lane & 31, hi = lane >> 5;
  int bm = blockIdx.y * 64, bn = blockIdx.x * 64;
  int wm = (wid >> 1) * 32, wn = (wid & 1) * 32;
  f32x16 acc = {};
  for (int k0 = 0; k0 < K; k0 += 32) {
    {
      int r = t >> 2, cc = (t & 3) * 8;
      *reinterpret_cast<bf16x8*>(&sA[r][cc]) = *reinterpret_cast<const bf16x8*>(A + (long)(bm + r) * K + k0 + cc);
    }
    {
      int kr = t >> 3, cc = (t & 7) * 8;
      bf16x8 v = {};
      if (bn + cc < N) v = *reinterpret_cast<const bf16x8*>(B + (long)(k0 + kr) * N + bn + cc);
      for (int j = 0; j < 8; ++j) sB[cc + j][kr] = (unsigned short)v[j];
    }
    __syncthreads();
    for (int s = 0; s < 2; ++s) {
      bf16x8 a = *reinterpret_cast<const bf16x8*>(&sA[wm + r32][s * 16 + hi * 8]);
      bf16x8 b = *reinterpret_cast<const bf16x8*>(&sB[wn + r32][s * 16 + hi * 8]);
      acc = __builtin_amdgcn_mfma_f32_32x32x16_bf16(a, b, acc, 0, 0, 0);
    }
    __syncthreads();
  }
  int n = bn + wn + r32;
  if (n < N) {
    for (int r = 0; r < 16; ++r) {
      int m = bm + wm + (r & 3) + 8 * (r >> 2) + 4 * hi;
      float v = rbf(acc[r]);
      if (EPI == 1) v = bf2f(res[(long)m * N + n]) + rbf(g[n] * v);
      C[(long)m * N + n] = f2bf(v);
    }
  }
}

__global__ __launch_bounds__(256) void k_geglu(const unsigned short* __restrict__ g, const unsigned short* __restrict__ u, unsigned short* __restrict__ o, int n) {
  int i = blockIdx.x * 256 + threadIdx.x;
  if (i < n) {
    float x = bf2f(g[i]);
    float inner = 0.7978845608028654f * (x + 0.044715f * x * x * x);
    float ge = rbf(0.5f * x * (1.f + tanhf(inner)));
    o[i] = f2bf(ge * bf2f(u[i]));
  }
}

constexpr int   D = 128, NW = 8, QBLK = 32, KVBLK = 64;
constexpr float SCALE = 0.088388347648318440f;
constexpr float THR = 8.f;
constexpr int SDEPTH = 2;
constexpr int LDQ = DQ, LDK = D, LDO = DQ;
constexpr size_t SHM_V = KVBLK * D * 2, SHM_K = KVBLK * D * 2, SHM_ATTN = 2 * SHM_V + 2 * SHM_K + NW * 64 * 4;
#define KSWZ(row, colB) ((row) * 256 + ((colB) ^ (((row) & 7) << 4)))
#define SBAR() __builtin_amdgcn_sched_barrier(0)
__device__ __forceinline__ int crow(int r, int hi) { return (r & 3) + 8 * (r >> 2) + 4 * hi; }
__device__ __forceinline__ unsigned cvtpk(float lo, float hi) {
  unsigned r; asm volatile("v_cvt_pk_bf16_f32 %0, %1, %2" : "=v"(r) : "v"(lo), "v"(hi)); return r;
}
__device__ __forceinline__ void partialSM(f32x16& p0, f32x16& p1, float& m_reg, float& mn, float& alpha) {
  constexpr float C = SCALE * 1.4426950408889634f;
  float pmax = p0[0]; for (int r = 1; r < 16; ++r) pmax = fmaxf(pmax, p0[r]); for (int r = 0; r < 16; ++r) pmax = fmaxf(pmax, p1[r]);
  { auto rr = __builtin_amdgcn_permlane32_swap(__float_as_uint(pmax), __float_as_uint(pmax), false, false);
    pmax = fmaxf(__uint_as_float(rr[0]), __uint_as_float(rr[1])); }
  if (__builtin_expect(__all(pmax - m_reg <= THR / SCALE), 1)) { mn = m_reg; alpha = 1.f; }
  else { mn = fmaxf(m_reg, pmax); alpha = __builtin_amdgcn_exp2f((m_reg - mn) * C); m_reg = mn; }
  float mnC = -mn * C;
  for (int r = 0; r < 16; ++r) p0[r] = fmaf(p0[r], C, mnC); for (int r = 0; r < 16; ++r) p1[r] = fmaf(p1[r], C, mnC);
  for (int r = 0; r < 16; ++r) p0[r] = __builtin_amdgcn_exp2f(p0[r]);
}
__device__ __forceinline__ void finishSM(f32x16& p0, f32x16& p1, float alpha, float& l_reg, bf16x8& pa0, bf16x8& pa1, bf16x8& pa2, bf16x8& pa3) {
  for (int r = 0; r < 16; ++r) p1[r] = __builtin_amdgcn_exp2f(p1[r]);
  float ps = 0; for (int r = 0; r < 16; ++r) ps += p0[r]; for (int r = 0; r < 16; ++r) ps += p1[r];
  { auto rr = __builtin_amdgcn_permlane32_swap(__float_as_uint(ps), __float_as_uint(ps), false, false);
    ps = __uint_as_float(rr[0]) + __uint_as_float(rr[1]); }
  l_reg = l_reg * alpha + ps;
#define PK4(P, BASE, OUT) do { unsigned a0 = cvtpk(P[BASE + 0], P[BASE + 1]), a1 = cvtpk(P[BASE + 2], P[BASE + 3]);   \
    unsigned b0 = cvtpk(P[BASE + 4], P[BASE + 5]), b1 = cvtpk(P[BASE + 6], P[BASE + 7]);                              \
    auto r0 = __builtin_amdgcn_permlane32_swap(a0, b0, false, false); auto r1 = __builtin_amdgcn_permlane32_swap(a1, b1, false, false); \
    u32x4 w = {r0[0], r1[0], r0[1], r1[1]}; OUT = *reinterpret_cast<bf16x8*>(&w); } while (0)
  PK4(p0, 0, pa0); PK4(p0, 8, pa1); PK4(p1, 0, pa2); PK4(p1, 8, pa3);
#undef PK4
}
__device__ __forceinline__ void qkt(f32x16& p0, f32x16& p1, const unsigned short* Ks, const bf16x8* qr, int r32, int hi) {
  p0 = f32x16{}; p1 = f32x16{};
  for (int d0 = 0; d0 < 8; ++d0) { int cb = (d0 * 16 + hi * 8) * 2;
    bf16x8 b0 = *reinterpret_cast<const bf16x8*>((const char*)Ks + KSWZ(r32, cb));
    bf16x8 b1 = *reinterpret_cast<const bf16x8*>((const char*)Ks + KSWZ(32 + r32, cb));
    p0 = __builtin_amdgcn_mfma_f32_32x32x16_bf16(b0, qr[d0], p0, 0, 0, 0);
    p1 = __builtin_amdgcn_mfma_f32_32x32x16_bf16(b1, qr[d0], p1, 0, 0, 0); }
}
__device__ __forceinline__ int v_st(int k, int c) { const int kk = (k & ~0xC) | ((k & 4) << 1) | ((k & 8) >> 1); return ((kk >> 3) * 4 + (c >> 5)) * 512 + ((kk & 7) * 32 + (c & 31)) * 2; }
__device__ __forceinline__ int v_rd_base(int lane) { return ((lane & 3) << 3) | (((lane >> 2) & 3) << 6) | (((lane >> 4) & 1) << 5) | (((lane >> 5) & 1) << 8); }
constexpr int v_rd_off(int d0, int ks, int half) { return d0 * 512 + ks * 4096 + half * 2048; }
template <int OFF> __device__ __forceinline__ s16x4 tr_read(int vb) {
  s16x4 r; asm volatile("ds_read_b64_tr_b16 %0, %1 offset:%2" : "=&v"(r) : "v"(vb), "i"(OFF) : "memory"); return r;
}
template <int D0> __device__ __forceinline__ void pv_one(f32x16& od, int vb, bf16x8 pa0, bf16x8 pa1, bf16x8 pa2, bf16x8 pa3) {
  const s16x4 l0 = tr_read<v_rd_off(D0, 0, 0)>(vb), h0 = tr_read<v_rd_off(D0, 0, 1)>(vb), l1 = tr_read<v_rd_off(D0, 1, 0)>(vb), h1 = tr_read<v_rd_off(D0, 1, 1)>(vb);
  const s16x4 l2 = tr_read<v_rd_off(D0, 2, 0)>(vb), h2 = tr_read<v_rd_off(D0, 2, 1)>(vb), l3 = tr_read<v_rd_off(D0, 3, 0)>(vb), h3 = tr_read<v_rd_off(D0, 3, 1)>(vb);
  asm volatile("s_waitcnt lgkmcnt(0)" ::: "memory"); SBAR();
#define PK(L, H) (bf16x8){L[0], L[1], L[2], L[3], H[0], H[1], H[2], H[3]}
  od = __builtin_amdgcn_mfma_f32_32x32x16_bf16(pa0, PK(l0, h0), od, 0, 0, 0);
  od = __builtin_amdgcn_mfma_f32_32x32x16_bf16(pa1, PK(l1, h1), od, 0, 0, 0);
  od = __builtin_amdgcn_mfma_f32_32x32x16_bf16(pa2, PK(l2, h2), od, 0, 0, 0);
  od = __builtin_amdgcn_mfma_f32_32x32x16_bf16(pa3, PK(l3, h3), od, 0, 0, 0);
#undef PK
}
__device__ __forceinline__ void pv_d0(f32x16* o, int vb, bf16x8 pa0, bf16x8 pa1, bf16x8 pa2, bf16x8 pa3) {
  pv_one<0>(o[0], vb, pa0, pa1, pa2, pa3); pv_one<1>(o[1], vb, pa0, pa1, pa2, pa3); pv_one<2>(o[2], vb, pa0, pa1, pa2, pa3); pv_one<3>(o[3], vb, pa0, pa1, pa2, pa3);
}
__device__ __forceinline__ void attn_dense_body(const unsigned short* __restrict__ Qb, const unsigned short* __restrict__ Kh, const unsigned short* __restrict__ Vh,
                                                unsigned short* __restrict__ Ob, int seq, char* lds) {
  const int tid = threadIdx.x, wid = tid >> 6, lane = tid & 63, r32 = lane & 31, hi = lane >> 5;
  unsigned short* V_lds = (unsigned short*)lds; unsigned short* K_lds = (unsigned short*)(lds + 2 * SHM_V);
  float* ws = (float*)(lds + 2 * SHM_V + 2 * SHM_K) + wid * 64; float* li_l = ws; float* al_l = ws + 32;
  float m_reg = -1e30f, l_reg = 0; f32x16 o[4] = {}; bf16x8 qr[8];
  const unsigned short* Qw = Qb + (long)(wid * QBLK + r32) * LDQ + hi * 8;
#pragma unroll
  for (int d0 = 0; d0 < 8; ++d0) qr[d0] = *reinterpret_cast<const bf16x8*>(Qw + d0 * 16);
  const int sr = tid >> 4, sc = (tid & 15) * 8, vst0 = v_st(sr, sc), vst1 = v_st(32 + sr, sc);
  const int vb0 = (int)(uintptr_t)V_lds + v_rd_base(lane);
  struct { bf16x8 vs0, vs1, ks0, ks1; } sr_[SDEPTH];
#define LD8(p) (*reinterpret_cast<const bf16x8*>(p))
#define SLOAD(i, k0) do { sr_[i].vs0 = LD8(&Vh[(long)((k0) + sr) * LDK + sc]); sr_[i].vs1 = LD8(&Vh[(long)((k0) + 32 + sr) * LDK + sc]); \
    sr_[i].ks0 = LD8(&Kh[(long)((k0) + sr) * LDK + sc]); sr_[i].ks1 = LD8(&Kh[(long)((k0) + 32 + sr) * LDK + sc]); } while (0)
#define SWRITE(b, i) do { *(bf16x8*)((char*)V_lds + (b) * SHM_V + vst0) = sr_[i].vs0;          \
    *(bf16x8*)((char*)V_lds + (b) * SHM_V + vst1) = sr_[i].vs1; int kc = sc * 2;               \
    *(bf16x8*)((char*)K_lds + (b) * SHM_K + KSWZ(sr, kc)) = sr_[i].ks0;                       \
    *(bf16x8*)((char*)K_lds + (b) * SHM_K + KSWZ(32 + sr, kc)) = sr_[i].ks1; } while (0)
#define SWAIT() do { asm volatile("s_waitcnt vmcnt(4)" ::: "memory"); } while (0)
#define RESC(a) do { if (__any((a) < 1.f)) { if (hi == 0) al_l[r32] = (a); asm volatile("s_waitcnt lgkmcnt(0)" ::: "memory"); \
    for (int d = 0; d < 4; ++d) for (int r = 0; r < 16; ++r) o[d][r] *= al_l[crow(r, hi)]; } } while (0)
  f32x16 pA0, pA1, pB0, pB1; float mnA, mnB, alA, alB; bf16x8 pa0, pa1, pa2, pa3; const int NT = seq / KVBLK;
  constexpr int SE = 0, SO = SDEPTH - 1;
  SLOAD(SE, 0); asm volatile("s_waitcnt vmcnt(0)" ::: "memory"); SWRITE(0, SE); __syncthreads();
  qkt(pA0, pA1, K_lds, qr, r32, hi); partialSM(pA0, pA1, m_reg, mnA, alA);
  SLOAD(SO, KVBLK); if (2 < NT) SLOAD(SE, 2 * KVBLK);
  SWAIT(); SWRITE(1, SO); __syncthreads();
  for (int j = 1; j + 1 < NT; j += 2) {
    SBAR(); qkt(pB0, pB1, (unsigned short*)((char*)K_lds + SHM_K), qr, r32, hi);
    finishSM(pA0, pA1, alA, l_reg, pa0, pa1, pa2, pa3); SBAR();
    SLOAD(SO, (j + SDEPTH) * KVBLK); SBAR();
    pv_d0(o, vb0, pa0, pa1, pa2, pa3); partialSM(pB0, pB1, m_reg, mnB, alB);
    __syncthreads(); SWAIT(); SWRITE(0, SE);
    RESC(alB); __syncthreads();
    SBAR(); qkt(pA0, pA1, K_lds, qr, r32, hi);
    finishSM(pB0, pB1, alB, l_reg, pa0, pa1, pa2, pa3); SBAR();
    if (j + 3 < NT) SLOAD(SE, (j + 1 + SDEPTH) * KVBLK); SBAR();
    pv_d0(o, vb0 + (int)SHM_V, pa0, pa1, pa2, pa3); partialSM(pA0, pA1, m_reg, mnA, alA);
    __syncthreads(); SWAIT(); SWRITE(1, SO);
    RESC(alA); __syncthreads();
  }
  SBAR(); qkt(pB0, pB1, (unsigned short*)((char*)K_lds + SHM_K), qr, r32, hi);
  finishSM(pA0, pA1, alA, l_reg, pa0, pa1, pa2, pa3); SBAR();
  pv_d0(o, vb0, pa0, pa1, pa2, pa3); partialSM(pB0, pB1, m_reg, mnB, alB);
  __syncthreads(); RESC(alB);
  finishSM(pB0, pB1, alB, l_reg, pa0, pa1, pa2, pa3); SBAR();
  pv_d0(o, vb0 + (int)SHM_V, pa0, pa1, pa2, pa3);
  if (hi == 0) li_l[r32] = l_reg; asm volatile("s_waitcnt lgkmcnt(0)" ::: "memory");
  float rli[16];
#pragma unroll
  for (int r = 0; r < 16; ++r) rli[r] = __builtin_amdgcn_rcpf(li_l[crow(r, hi)]);
  unsigned short* Ow = Ob + (long)(wid * QBLK) * LDO;
#pragma unroll
  for (int r = 0; r < 16; ++r) { int orow = crow(r, hi);
    for (int d0 = 0; d0 < 4; ++d0) Ow[(long)orow * LDO + d0 * 32 + r32] = f2bf(o[d0][r] * rli[r]); }
}
__global__ __launch_bounds__(64 * NW, 1)
void k_attn(const unsigned short* __restrict__ Q, const unsigned short* __restrict__ K, const unsigned short* __restrict__ V, unsigned short* __restrict__ O) {
  extern __shared__ __attribute__((aligned(16))) char shm[];
  const long h = blockIdx.y, i0 = (long)blockIdx.x * (QBLK * NW);
  attn_dense_body(Q + i0 * LDQ + h * D, K + h * SEQ * (long)D, V + h * SEQ * (long)D, O + i0 * LDO + h * D, SEQ, shm);
}

extern "C" void kernel_launch(void* const* d_in, const int* in_sizes, int n_in, void* d_out, int out_size, void* d_ws, size_t ws_size, hipStream_t stream) {
  const unsigned short* x = (const unsigned short*)d_in[0];
  const unsigned short* kc = (const unsigned short*)d_in[1];
  const unsigned short* vc = (const unsigned short*)d_in[2];
  const unsigned short* wq = (const unsigned short*)d_in[3];
  const unsigned short* wo = (const unsigned short*)d_in[4];
  const unsigned short* wg = (const unsigned short*)d_in[6];
  const unsigned short* wu = (const unsigned short*)d_in[7];
  const unsigned short* wd = (const unsigned short*)d_in[8];
  const unsigned short* c = (const unsigned short*)d_in[9];
  const unsigned short* wm = (const unsigned short*)d_in[10];
  char* ws = (char*)d_ws;
  float* mod = (float*)ws;
  unsigned short* h  = (unsigned short*)(ws + (1 << 20));
  unsigned short* q  = (unsigned short*)(ws + (4 << 20));
  unsigned short* o  = (unsigned short*)(ws + (20 << 20));
  unsigned short* x1 = (unsigned short*)(ws + (36 << 20));
  unsigned short* g  = (unsigned short*)(ws + (40 << 20));
  unsigned short* u  = (unsigned short*)(ws + (48 << 20));
  unsigned short* a  = (unsigned short*)(ws + (56 << 20));
  hipFuncSetAttribute((const void*)k_attn, hipFuncAttributeMaxDynamicSharedMemorySize, (int)SHM_ATTN);
  k_mod<<<48, 256, 0, stream>>>(c, wm, mod);
  k_ln<<<SEQ / 4, 256, 0, stream>>>(x, mod, mod + 512, h);
  k_gemm<0><<<dim3(DQ / 64, SEQ / 64), 256, 0, stream>>>(h, wq, q, SEQ, DQ, DM, nullptr, nullptr);
  k_attn<<<dim3(SEQ / 256, NH), 512, SHM_ATTN, stream>>>(q, kc, vc, o);
  k_gemm<1><<<dim3(DM / 64, SEQ / 64), 256, 0, stream>>>(o, wo, x1, SEQ, DM, DQ, x, mod + 1024);
  k_ln<<<SEQ / 4, 256, 0, stream>>>(x1, mod + 1536, mod + 2048, h);
  k_gemm<0><<<dim3((DF + 63) / 64, SEQ / 64), 256, 0, stream>>>(h, wg, g, SEQ, DF, DM, nullptr, nullptr);
  k_gemm<0><<<dim3((DF + 63) / 64, SEQ / 64), 256, 0, stream>>>(h, wu, u, SEQ, DF, DM, nullptr, nullptr);
  k_geglu<<<(SEQ * DF + 255) / 256, 256, 0, stream>>>(g, u, a, SEQ * DF);
  k_gemm<1><<<dim3(DM / 64, SEQ / 64), 256, 0, stream>>>(a, wd, (unsigned short*)d_out, SEQ, DM, DF, x1, mod + 2560);
}
```

```cpp
#include <hip/hip_runtime.h>
#include <hip/hip_bf16.h>
#include <utility>

constexpr int SEQ = 2048, DM = 512, NH = 32, DH = 128, DQ = 4096, DF = 1376;
constexpr int D = 128, NWAVE = 4, QROWS = 64, KVBLK = 64;
#define ATTN_THR 40.0f
constexpr float THR = ATTN_THR;
constexpr int NT = SEQ / KVBLK;
constexpr unsigned KRS = 256u, KTILEB = 64u * KRS;
constexpr size_t SHM_ATTN = 163840;

typedef float    f32x16 __attribute__((ext_vector_type(16)));
typedef float    f32x2  __attribute__((ext_vector_type(2)));
typedef float    f32x4  __attribute__((ext_vector_type(4)));
typedef __bf16   bf16x8 __attribute__((ext_vector_type(8)));
typedef __bf16   bf16x4 __attribute__((ext_vector_type(4)));
typedef __bf16   bf16x2 __attribute__((ext_vector_type(2)));
typedef unsigned u32x4  __attribute__((ext_vector_type(4)));
typedef unsigned u32x2  __attribute__((ext_vector_type(2)));
template <int... Is> using iseq = std::integer_sequence<int, Is...>;
template <int N> using make_iseq = std::make_integer_sequence<int, N>;

#define KRING 0x0000u
#define VRING 0x8000u
#define QSTAGE 0x10000u
#define OSTAGE 0x20000u
#define SRD_WORD3 0x00020000
#define DI __device__ __forceinline__
#define GAP() __builtin_amdgcn_sched_barrier(0)
#define W4_ACLOB_HI "a128","a129","a130","a131","a132","a133","a134","a135","a136","a137","a138","a139","a140","a141","a142","a143","a144","a145","a146","a147","a148","a149","a150","a151","a152","a153","a154","a155","a156","a157","a158","a159","a160","a161","a162","a163","a164","a165","a166","a167","a168","a169","a170","a171","a172","a173","a174","a175","a176","a177","a178","a179","a180","a181","a182","a183","a184","a185","a186","a187","a188","a189","a190","a191","a192","a193","a194","a195","a196","a197","a198","a199","a200","a201","a202","a203","a204","a205","a206","a207","a208","a209","a210","a211","a212","a213","a214","a215","a216","a217","a218","a219","a220","a221","a222","a223","a224","a225","a226","a227","a228","a229","a230","a231","a232","a233","a234","a235","a236","a237","a238","a239","a240","a241","a242","a243","a244","a245","a246","a247","a248","a249","a250","a251","a252","a253","a254","a255"
#define W4_ACLOB "a0","a1","a2","a3","a4","a5","a6","a7","a8","a9","a10","a11","a12","a13","a14","a15","a16","a17","a18","a19","a20","a21","a22","a23","a24","a25","a26","a27","a28","a29","a30","a31","a32","a33","a34","a35","a36","a37","a38","a39","a40","a41","a42","a43","a44","a45","a46","a47","a48","a49","a50","a51","a52","a53","a54","a55","a56","a57","a58","a59","a60","a61","a62","a63","a64","a65","a66","a67","a68","a69","a70","a71","a72","a73","a74","a75","a76","a77","a78","a79","a80","a81","a82","a83","a84","a85","a86","a87","a88","a89","a90","a91","a92","a93","a94","a95","a96","a97","a98","a99","a100","a101","a102","a103","a104","a105","a106","a107","a108","a109","a110","a111","a112","a113","a114","a115","a116","a117","a118","a119","a120","a121","a122","a123","a124","a125","a126","a127"
#define W4_READ_O(DB, QB, o) do { switch (2 * (DB) + (QB)) { \
    case 0: asm volatile("v_accvgpr_read_b32 %0, a0\n\t" "v_accvgpr_read_b32 %1, a1\n\t" "v_accvgpr_read_b32 %2, a2\n\t" "v_accvgpr_read_b32 %3, a3\n\t" "v_accvgpr_read_b32 %4, a4\n\t" "v_accvgpr_read_b32 %5, a5\n\t" "v_accvgpr_read_b32 %6, a6\n\t" "v_accvgpr_read_b32 %7, a7\n\t" "v_accvgpr_read_b32 %8, a8\n\t" "v_accvgpr_read_b32 %9, a9\n\t" "v_accvgpr_read_b32 %10, a10\n\t" "v_accvgpr_read_b32 %11, a11\n\t" "v_accvgpr_read_b32 %12, a12\n\t" "v_accvgpr_read_b32 %13, a13\n\t" "v_accvgpr_read_b32 %14, a14\n\t" "v_accvgpr_read_b32 %15, a15\n\t" : "=v"((o)[0]), "=v"((o)[1]), "=v"((o)[2]), "=v"((o)[3]), "=v"((o)[4]), "=v"((o)[5]), "=v"((o)[6]), "=v"((o)[7]), "=v"((o)[8]), "=v"((o)[9]), "=v"((o)[10]), "=v"((o)[11]), "=v"((o)[12]), "=v"((o)[13]), "=v"((o)[14]), "=v"((o)[15])); break; \
    case 1: asm volatile("v_accvgpr_read_b32 %0, a16\n\t" "v_accvgpr_read_b32 %1, a17\n\t" "v_accvgpr_read_b32 %2, a18\n\t" "v_accvgpr_read_b32 %3, a19\n\t" "v_accvgpr_read_b32 %4, a20\n\t" "v_accvgpr_read_b32 %5, a21\n\t" "v_accvgpr_read_b32 %6, a22\n\t" "v_accvgpr_read_b32 %7, a23\n\t" "v_accvgpr_read_b32 %8, a24\n\t" "v_accvgpr_read_b32 %9, a25\n\t" "v_accvgpr_read_b32 %10, a26\n\t" "v_accvgpr_read_b32 %11, a27\n\t" "v_accvgpr_read_b32 %12, a28\n\t" "v_accvgpr_read_b32 %13, a29\n\t" "v_accvgpr_read_b32 %14, a30\n\t" "v_accvgpr_read_b32 %15, a31\n\t" : "=v"((o)[0]), "=v"((o)[1]), "=v"((o)[2]), "=v"((o)[3]), "=v"((o)[4]), "=v"((o)[5]), "=v"((o)[6]), "=v"((o)[7]), "=v"((o)[8]), "=v"((o)[9]), "=v"((o)[10]), "=v"((o)[11]), "=v"((o)[12]), "=v"((o)[13]), "=v"((o)[14]), "=v"((o)[15])); break; \
    case 2: asm volatile("v_accvgpr_read_b32 %0, a32\n\t" "v_accvgpr_read_b32 %1, a33\n\t" "v_accvgpr_read_b32 %2, a34\n\t" "v_accvgpr_read_b32 %3, a35\n\t" "v_accvgpr_read_b32 %4, a36\n\t" "v_accvgpr_read_b32 %5, a37\n\t" "v_accvgpr_read_b32 %6, a38\n\t" "v_accvgpr_read_b32 %7, a39\n\t" "v_accvgpr_read_b32 %8, a40\n\t" "v_accvgpr_read_b32 %9, a41\n\t" "v_accvgpr_read_b32 %10, a42\n\t" "v_accvgpr_read_b32 %11, a43\n\t" "v_accvgpr_read_b32 %12, a44\n\t" "v_accvgpr_read_b32 %13, a45\n\t" "v_accvgpr_read_b32 %14, a46\n\t" "v_accvgpr_read_b32 %15, a47\n\t" : "=v"((o)[0]), "=v"((o)[1]), "=v"((o)[2]), "=v"((o)[3]), "=v"((o)[4]), "=v"((o)[5]), "=v"((o)[6]), "=v"((o)[7]), "=v"((o)[8]), "=v"((o)[9]), "=v"((o)[10]), "=v"((o)[11]), "=v"((o)[12]), "=v"((o)[13]), "=v"((o)[14]), "=v"((o)[15])); break; \
    case 3: asm volatile("v_accvgpr_read_b32 %0, a48\n\t" "v_accvgpr_read_b32 %1, a49\n\t" "v_accvgpr_read_b32 %2, a50\n\t" "v_accvgpr_read_b32 %3, a51\n\t" "v_accvgpr_read_b32 %4, a52\n\t" "v_accvgpr_read_b32 %5, a53\n\t" "v_accvgpr_read_b32 %6, a54\n\t" "v_accvgpr_read_b32 %7, a55\n\t" "v_accvgpr_read_b32 %8, a56\n\t" "v_accvgpr_read_b32 %9, a57\n\t" "v_accvgpr_read_b32 %10, a58\n\t" "v_accvgpr_read_b32 %11, a59\n\t" "v_accvgpr_read_b32 %12, a60\n\t" "v_accvgpr_read_b32 %13, a61\n\t" "v_accvgpr_read_b32 %14, a62\n\t" "v_accvgpr_read_b32 %15, a63\n\t" : "=v"((o)[0]), "=v"((o)[1]), "=v"((o)[2]), "=v"((o)[3]), "=v"((o)[4]), "=v"((o)[5]), "=v"((o)[6]), "=v"((o)[7]), "=v"((o)[8]), "=v"((o)[9]), "=v"((o)[10]), "=v"((o)[11]), "=v"((o)[12]), "=v"((o)[13]), "=v"((o)[14]), "=v"((o)[15])); break; \
    case 4: asm volatile("v_accvgpr_read_b32 %0, a64\n\t" "v_accvgpr_read_b32 %1, a65\n\t" "v_accvgpr_read_b32 %2, a66\n\t" "v_accvgpr_read_b32 %3, a67\n\t" "v_accvgpr_read_b32 %4, a68\n\t" "v_accvgpr_read_b32 %5, a69\n\t" "v_accvgpr_read_b32 %6, a70\n\t" "v_accvgpr_read_b32 %7, a71\n\t" "v_accvgpr_read_b32 %8, a72\n\t" "v_accvgpr_read_b32 %9, a73\n\t" "v_accvgpr_read_b32 %10, a74\n\t" "v_accvgpr_read_b32 %11, a75\n\t" "v_accvgpr_read_b32 %12, a76\n\t" "v_accvgpr_read_b32 %13, a77\n\t" "v_accvgpr_read_b32 %14, a78\n\t" "v_accvgpr_read_b32 %15, a79\n\t" : "=v"((o)[0]), "=v"((o)[1]), "=v"((o)[2]), "=v"((o)[3]), "=v"((o)[4]), "=v"((o)[5]), "=v"((o)[6]), "=v"((o)[7]), "=v"((o)[8]), "=v"((o)[9]), "=v"((o)[10]), "=v"((o)[11]), "=v"((o)[12]), "=v"((o)[13]), "=v"((o)[14]), "=v"((o)[15])); break; \
    case 5: asm volatile("v_accvgpr_read_b32 %0, a80\n\t" "v_accvgpr_read_b32 %1, a81\n\t" "v_accvgpr_read_b32 %2, a82\n\t" "v_accvgpr_read_b32 %3, a83\n\t" "v_accvgpr_read_b32 %4, a84\n\t" "v_accvgpr_read_b32 %5, a85\n\t" "v_accvgpr_read_b32 %6, a86\n\t" "v_accvgpr_read_b32 %7, a87\n\t" "v_accvgpr_read_b32 %8, a88\n\t" "v_accvgpr_read_b32 %9, a89\n\t" "v_accvgpr_read_b32 %10, a90\n\t" "v_accvgpr_read_b32 %11, a91\n\t" "v_accvgpr_read_b32 %12, a92\n\t" "v_accvgpr_read_b32 %13, a93\n\t" "v_accvgpr_read_b32 %14, a94\n\t" "v_accvgpr_read_b32 %15, a95\n\t" : "=v"((o)[0]), "=v"((o)[1]), "=v"((o)[2]), "=v"((o)[3]), "=v"((o)[4]), "=v"((o)[5]), "=v"((o)[6]), "=v"((o)[7]), "=v"((o)[8]), "=v"((o)[9]), "=v"((o)[10]), "=v"((o)[11]), "=v"((o)[12]), "=v"((o)[13]), "=v"((o)[14]), "=v"((o)[15])); break; \
    case 6: asm volatile("v_accvgpr_read_b32 %0, a96\n\t" "v_accvgpr_read_b32 %1, a97\n\t" "v_accvgpr_read_b32 %2, a98\n\t" "v_accvgpr_read_b32 %3, a99\n\t" "v_accvgpr_read_b32 %4, a100\n\t" "v_accvgpr_read_b32 %5, a101\n\t" "v_accvgpr_read_b32 %6, a102\n\t" "v_accvgpr_read_b32 %7, a103\n\t" "v_accvgpr_read_b32 %8, a104\n\t" "v_accvgpr_read_b32 %9, a105\n\t" "v_accvgpr_read_b32 %10, a106\n\t" "v_accvgpr_read_b32 %11, a107\n\t" "v_accvgpr_read_b32 %12, a108\n\t" "v_accvgpr_read_b32 %13, a109\n\t" "v_accvgpr_read_b32 %14, a110\n\t" "v_accvgpr_read_b32 %15, a111\n\t" : "=v"((o)[0]), "=v"((o)[1]), "=v"((o)[2]), "=v"((o)[3]), "=v"((o)[4]), "=v"((o)[5]), "=v"((o)[6]), "=v"((o)[7]), "=v"((o)[8]), "=v"((o)[9]), "=v"((o)[10]), "=v"((o)[11]), "=v"((o)[12]), "=v"((o)[13]), "=v"((o)[14]), "=v"((o)[15])); break; \
    case 7: asm volatile("v_accvgpr_read_b32 %0, a112\n\t" "v_accvgpr_read_b32 %1, a113\n\t" "v_accvgpr_read_b32 %2, a114\n\t" "v_accvgpr_read_b32 %3, a115\n\t" "v_accvgpr_read_b32 %4, a116\n\t" "v_accvgpr_read_b32 %5, a117\n\t" "v_accvgpr_read_b32 %6, a118\n\t" "v_accvgpr_read_b32 %7, a119\n\t" "v_accvgpr_read_b32 %8, a120\n\t" "v_accvgpr_read_b32 %9, a121\n\t" "v_accvgpr_read_b32 %10, a122\n\t" "v_accvgpr_read_b32 %11, a123\n\t" "v_accvgpr_read_b32 %12, a124\n\t" "v_accvgpr_read_b32 %13, a125\n\t" "v_accvgpr_read_b32 %14, a126\n\t" "v_accvgpr_read_b32 %15, a127\n\t" : "=v"((o)[0]), "=v"((o)[1]), "=v"((o)[2]), "=v"((o)[3]), "=v"((o)[4]), "=v"((o)[5]), "=v"((o)[6]), "=v"((o)[7]), "=v"((o)[8]), "=v"((o)[9]), "=v"((o)[10]), "=v"((o)[11]), "=v"((o)[12]), "=v"((o)[13]), "=v"((o)[14]), "=v"((o)[15])); break; \
    } } while (0)

DI unsigned rfl(unsigned x) { return __builtin_amdgcn_readfirstlane(x); }
DI unsigned cvtpk(float lo, float hi) {
  f32x2 v = {lo, hi}; bf16x2 b = __builtin_convertvector(v, bf16x2); return __builtin_bit_cast(unsigned, b);
}
DI float max3f(float a, float b, float c) { float r; asm("v_max3_f32 %0, %1, %2, %3" : "=v"(r) : "v"(a), "v"(b), "v"(c)); return r; }
DI float max2f(float a, float b) { float r; asm("v_max_f32_e32 %0, %1, %2" : "=v"(r) : "v"(a), "v"(b)); return r; }
DI float fadd_s(float a, float b) { float r; asm("v_add_f32_e32 %0, %1, %2" : "=v"(r) : "v"(a), "v"(b)); return r; }
DI float fmul_s(float a, float b) { float r; asm("v_mul_f32_e32 %0, %1, %2" : "=v"(r) : "v"(a), "v"(b)); return r; }
DI float fsub_s(float a, float b) { float r; asm("v_sub_f32_e32 %0, %1, %2" : "=v"(r) : "v"(a), "v"(b)); return r; }
struct W4C {
  unsigned vkb[4], vvb[2], vksrc[4], vvsrc, vvsrc2[2], m0k, m0v, sofk, sofv, ldsb;
  __amdgpu_buffer_rsrc_t srdK, srdV, srdV2;
  int T; float THR;
  f32x4 S[2][4][4];
  f32x4 NEGM[4];
  bf16x8 Vf[8][2];
  bf16x8 Pk[4][2];
  bf16x4 vlo[8][2], vup[8][2];
  float mhat[4], lsum[4], fct[4]; bool pend;
  float rs0[4], rs1[4]; int pmx0, pmx1;
  unsigned pm0[8], psf[8];
};
constexpr int AO_(int db, int qb) { return 16 * (2 * db + qb); }
constexpr int BO_(int db, int qb) { return 4 * (4 * db + qb); }
constexpr int BQ_(int qb, int ds) { return 128 + 16 * qb + 4 * ds; }
constexpr int BK_(int kb, int ds) { return 192 + 16 * kb + 4 * ds; }

template <int I> DI void tr_read(W4C& c, unsigned vslot) {
  constexpr int ks = I >> 4, db = (I >> 1) & 7;
  if constexpr ((I & 1) == 0) asm volatile("ds_read_b64_tr_b16 %0, %1 offset:%2" : "=v"(c.vlo[db][ks]) : "v"(c.vvb[db & 1] + vslot), "i"(8192 * ks + 512 * (db >> 1)) : "memory");
  else                        asm volatile("ds_read_b64_tr_b16 %0, %1 offset:%2" : "=v"(c.vup[db][ks]) : "v"(c.vvb[db & 1] + vslot), "i"(8192 * ks + 4096 + 512 * (db >> 1)) : "memory");
}
template <int I> DI void k_read(W4C& c, unsigned slot) {
  constexpr int ds = I >> 2, kb = I & 3;
  asm volatile("ds_read_b128 a[%c1:%c2], %0 offset:%c3" :: "v"(c.vkb[ds] + slot), "i"(BK_(kb, ds)), "i"(BK_(kb, ds) + 3), "i"(4096 * kb) : "memory", W4_ACLOB_HI);
}
template <int I> DI void q_read(W4C& c, unsigned qoff) {
  constexpr int ds = I >> 2, qb = I & 3;
  asm volatile("ds_read_b128 a[%c1:%c2], %0 offset:%c3" :: "v"(c.vkb[ds] + qoff), "i"(BQ_(qb, ds)), "i"(BQ_(qb, ds) + 3), "i"(4096 * qb) : "memory", W4_ACLOB_HI);
}
template <int X, bool FIRST, int N> DI void qk_mfma(W4C& c) {
  constexpr int ds = N >> 4, kb = (N >> 2) & 3, qb = N & 3, ak = BK_(kb, ds), aq = BQ_(qb, ds);
  if constexpr (ds == 0) {
    if constexpr (FIRST) asm volatile("v_mfma_f32_16x16x32_bf16 %0, a[%c1:%c2], a[%c3:%c4], 0" : "=&v"(c.S[X][kb][qb]) : "i"(ak), "i"(ak + 3), "i"(aq), "i"(aq + 3) : W4_ACLOB, W4_ACLOB_HI);
    else                 asm volatile("v_mfma_f32_16x16x32_bf16 %0, a[%c2:%c3], a[%c4:%c5], %1" : "=&v"(c.S[X][kb][qb]) : "v"(c.NEGM[qb]), "i"(ak), "i"(ak + 3), "i"(aq), "i"(aq + 3) : W4_ACLOB, W4_ACLOB_HI);
  } else asm volatile("v_mfma_f32_16x16x32_bf16 %0, a[%c1:%c2], a[%c3:%c4], %0" : "+v"(c.S[X][kb][qb]) : "i"(ak), "i"(ak + 3), "i"(aq), "i"(aq + 3) : W4_ACLOB, W4_ACLOB_HI);
}
template <int N, bool ZERO> DI void pv_mfma(W4C& c) {
  constexpr int ks = N >> 5, db = (N >> 2) & 7, qb = N & 3, ao = BO_(db, qb);
  if constexpr ((N & 7) == 0) {
    if constexpr (ZERO && ks == 0) asm volatile("v_mfma_f32_16x16x32_bf16 a[%c2:%c3], %0, %1, 0" :: "v"(c.Vf[db][ks]), "v"(c.Pk[qb][ks]), "i"(ao), "i"(ao + 3) : W4_ACLOB);
    else                         asm volatile("v_mfma_f32_16x16x32_bf16 a[%c2:%c3], %0, %1, a[%c2:%c3]" :: "v"(c.Vf[db][ks]), "v"(c.Pk[qb][ks]), "i"(ao), "i"(ao + 3) : W4_ACLOB);
  } else {
    if constexpr (ZERO && ks == 0) asm volatile("v_mfma_f32_16x16x32_bf16 a[%c2:%c3], %0, %1, 0" :: "v"(c.Vf[db][ks]), "v"(c.Pk[qb][ks]), "i"(ao), "i"(ao + 3));
    else                         asm volatile("v_mfma_f32_16x16x32_bf16 a[%c2:%c3], %0, %1, a[%c2:%c3]" :: "v"(c.Vf[db][ks]), "v"(c.Pk[qb][ks]), "i"(ao), "i"(ao + 3));
  }
}
#define SEL(X, H, E) c.S[X][2 * (H) + (((E) >> 2) & 1)][(E) >> 3][(E) & 3]
template <int X, int KS, int C> DI void pack1(W4C& c) {
  u32x4 w = __builtin_bit_cast(u32x4, c.Pk[C >> 2][KS]);
  unsigned dw = cvtpk(SEL(X, KS, 2 * C), SEL(X, KS, 2 * C + 1));
  asm volatile("" : "+v"(dw));
  w[C & 3] = dw;
  c.Pk[C >> 2][KS] = __builtin_bit_cast(bf16x8, w);
}
template <int X, int H, int E> DI void exp1(W4C& c) { SEL(X, H, E) = __builtin_amdgcn_exp2f(SEL(X, H, E)); }
template <int X, int H, int E> DI void sum1(W4C& c) {
  if constexpr (E & 1) c.rs1[E >> 3] = fadd_s(c.rs1[E >> 3], SEL(X, H, E)); else c.rs0[E >> 3] = fadd_s(c.rs0[E >> 3], SEL(X, H, E));
}

template <int X, bool FIRST, int M> DI void gapA(W4C& c, unsigned vslot, const int t) {
  constexpr int Xp = X ^ 1;
  qk_mfma<X, FIRST, 2 * M>(c); GAP();
  if constexpr (!FIRST) {
    constexpr int base = M + (M >= 4) + (M >= 8) + (M >= 12) + (M >= 16) + (M >= 20) + (M >= 24);
    if constexpr (M <= 25) tr_read<base>(c, vslot);
    exp1<Xp, 1, M>(c);
  }
  GAP();
  qk_mfma<X, FIRST, 2 * M + 1>(c); GAP();
  if constexpr (M == 28) { c.psf[0] = KTILEB * (unsigned)(t + 2) + c.sofk; asm volatile("" : "+s"(c.psf[0])); }
  if constexpr (M == 30) { c.psf[1] = KTILEB * (unsigned)(t + 1) + c.sofv; asm volatile("" : "+s"(c.psf[1])); }
  if constexpr (!FIRST && M == 12) asm volatile("" :: "v"(c.NEGM[0]), "v"(c.NEGM[1]), "v"(c.NEGM[2]), "v"(c.NEGM[3]));
  if constexpr (!FIRST) {
    constexpr int base = M + (M >= 4) + (M >= 8) + (M >= 12) + (M >= 16) + (M >= 20) + (M >= 24);
    if constexpr (M == 3 || M == 7 || M == 11 || M == 15 || M == 19 || M == 23) tr_read<base + 1>(c, vslot);
    if constexpr (M >= 1) sum1<Xp, 1, M - 1>(c);
    if constexpr (M < 16) pack1<Xp, 0, M>(c);
    else if constexpr (M >= 17) pack1<Xp, 1, M - 17>(c);
  }
  GAP();
}
#define W4_VLO "+v"(c.vlo[0][0]), "+v"(c.vlo[1][0]), "+v"(c.vlo[2][0]), "+v"(c.vlo[3][0]), "+v"(c.vlo[4][0]), "+v"(c.vlo[5][0]), "+v"(c.vlo[6][0]), "+v"(c.vlo[7][0]), \
               "+v"(c.vlo[0][1]), "+v"(c.vlo[1][1]), "+v"(c.vlo[2][1]), "+v"(c.vlo[3][1]), "+v"(c.vlo[4][1]), "+v"(c.vlo[5][1]), "+v"(c.vlo[6][1]), "+v"(c.vlo[7][1])
#define W4_VUP "+v"(c.vup[0][0]), "+v"(c.vup[1][0]), "+v"(c.vup[2][0]), "+v"(c.vup[3][0]), "+v"(c.vup[4][0]), "+v"(c.vup[5][0]), "+v"(c.vup[6][0]), "+v"(c.vup[7][0]), \
               "+v"(c.vup[0][1]), "+v"(c.vup[1][1]), "+v"(c.vup[2][1]), "+v"(c.vup[3][1]), "+v"(c.vup[4][1]), "+v"(c.vup[5][1]), "+v"(c.vup[6][1]), "+v"(c.vup[7][1])
DI void v_concat(W4C& c) {
  #pragma unroll
  for (int ks = 0; ks < 2; ++ks)
    #pragma unroll
    for (int db = 0; db < 8; ++db) c.Vf[db][ks] = __builtin_shufflevector(c.vlo[db][ks], c.vup[db][ks], 0, 1, 2, 3, 4, 5, 6, 7);
}
template <int X, bool FIRST, int... Ms> DI void phaseA_(W4C& c, const int t, iseq<Ms...>) {
  const unsigned vslot = (unsigned)((t - 1) & 1) * 0x4000u;
  (gapA<X, FIRST, Ms>(c, vslot, t), ...);
  if constexpr (!FIRST) {
    sum1<X ^ 1, 1, 31>(c);
    pack1<X ^ 1, 1, 15>(c);
  }
  if constexpr (FIRST) asm volatile("s_waitcnt vmcnt(0) lgkmcnt(0)\n\ts_barrier" ::: "memory");
  else { asm volatile("s_waitcnt vmcnt(0) lgkmcnt(0)" : W4_VLO :: "memory"); asm volatile("s_barrier" : W4_VUP :: "memory"); }
  GAP();
  if constexpr (!FIRST) v_concat(c);
}
template <int X, bool FIRST> DI void phaseA(W4C& c, const int t) { phaseA_<X, FIRST>(c, t, make_iseq<32>{}); }

#define SV(X, I) c.S[X][(I) >> 4][((I) >> 2) & 3][(I) & 3]
DI int imax3(int a, int b, int c) { return max(max(a, b), c); }
#define SI(X, I) __float_as_int(SV(X, I))
template <int X, int I> DI void rmax1(W4C& c) {
  if constexpr (I == 0) c.pmx0 = imax3(SI(X, 0), SI(X, 1), SI(X, 2));
  else if constexpr (I == 1) c.pmx1 = imax3(SI(X, 3), SI(X, 4), SI(X, 5));
  else if constexpr (I <= 30) { constexpr int i0 = 6 + 2 * (I - 2);
    if constexpr (I & 1) c.pmx1 = imax3(c.pmx1, SI(X, i0), SI(X, i0 + 1)); else c.pmx0 = imax3(c.pmx0, SI(X, i0), SI(X, i0 + 1)); }
  else if constexpr (I == 31) c.pmx0 = max(c.pmx0, c.pmx1);
  if constexpr (I == 0 || (I <= 31 && !(I & 1)) || I == 31) asm volatile("" :: "v"(c.pmx0)); else if constexpr (I <= 31) asm volatile("" :: "v"(c.pmx1));
}
template <int X, int Q> DI float rowmax(W4C& c) {
  float m = max3f(c.S[X][0][Q][0], c.S[X][0][Q][1], c.S[X][0][Q][2]);
  m = max3f(m, c.S[X][0][Q][3], c.S[X][1][Q][0]); m = max3f(m, c.S[X][1][Q][1], c.S[X][1][Q][2]); m = max3f(m, c.S[X][1][Q][3], c.S[X][2][Q][0]);
  m = max3f(m, c.S[X][2][Q][1], c.S[X][2][Q][2]); m = max3f(m, c.S[X][2][Q][3], c.S[X][3][Q][0]); m = max3f(m, c.S[X][3][Q][1], c.S[X][3][Q][2]);
  m = max2f(m, c.S[X][3][Q][3]);
  { auto r = __builtin_amdgcn_permlane16_swap(__float_as_uint(m), __float_as_uint(m), false, false); m = max2f(__uint_as_float(r[0]), __uint_as_float(r[1])); }
  { auto r = __builtin_amdgcn_permlane32_swap(__float_as_uint(m), __float_as_uint(m), false, false); m = max2f(__uint_as_float(r[0]), __uint_as_float(r[1])); }
  return m;
}
template <int X, bool PEEL, int Q> DI void newref(W4C& c) {
  const float rm = rowmax<X, Q>(c);
  const float dl = PEEL ? rm : max2f(rm, 0.f);
  if constexpr (PEEL) { c.mhat[Q] = rm; c.rs0[Q] = 0.f; c.rs1[Q] = 0.f; c.fct[Q] = 1.f; }
  else { c.mhat[Q] = fadd_s(c.mhat[Q], dl); c.fct[Q] = __builtin_amdgcn_exp2f(-dl);
         c.rs0[Q] *= c.fct[Q]; c.rs1[Q] *= c.fct[Q]; }
  #pragma unroll
  for (int kb = 0; kb < 4; ++kb) { _Pragma("unroll") for (int e = 0; e < 4; ++e) c.S[X][kb][Q][e] = fsub_s(c.S[X][kb][Q][e], dl); }
  const float nm = -c.mhat[Q];
  {
    unsigned l; asm volatile("v_mbcnt_lo_u32_b32 %0, -1, 0\n\tv_mbcnt_hi_u32_b32 %0, -1, %0" : "=v"(l));
    const float aone = l < 16u ? 1.f : 0.f;
    asm volatile("s_nop 1\n\tv_mfma_f32_16x16x4_f32 %0, %1, %2, 0" : "+v"(c.NEGM[Q]) : "v"(aone), "v"(nm));
  }
}
constexpr int BG_DEC = 11;
constexpr int BG_N = 31 - BG_DEC;
template <int X, bool PEEL, bool ZERO1, int DK, int DV, bool KR, int M> DI void gapB(W4C& c, const unsigned kslot_rd) {
  if constexpr (!PEEL) { pv_mfma<2 * M, ZERO1>(c); GAP(); }
  if constexpr (M % 3 == 0 && M < 24) {
    constexpr int q = M / 3;
    if constexpr (q == 0 && DK == 1) asm volatile("s_mov_b32 m0, %0\n\ts_nop 0\n\tbuffer_load_dwordx4 %1, %2, %3 offen lds" :: "s"(c.m0k + (unsigned)X * 0x4000u), "v"(c.vksrc[0]), "s"(c.srdK), "s"(c.psf[0]) : "m0", "memory");
    if constexpr (q > 0 && q < 4 && DK == 1) asm volatile("buffer_load_dwordx4 %0, %1, %2 offen offset:%c3 lds" :: "v"(c.vksrc[q & 3]), "s"(c.srdK), "s"(c.psf[0]), "i"(1024 * q) : "memory");
    if constexpr (q == 4 && DV == 1) asm volatile("s_mov_b32 m0, %0\n\ts_nop 0\n\tbuffer_load_dwordx4 %1, %2, %3 offen lds" :: "s"(c.m0v + (unsigned)(X ^ 1) * 0x4000u), "v"(c.vvsrc2[0]), "s"(c.srdV2), "s"(c.psf[1]) : "m0", "memory");
    if constexpr (q > 4 && DV == 1) asm volatile("buffer_load_dwordx4 %0, %1, %2 offen offset:%c3 lds" :: "v"(c.vvsrc2[q & 1]), "s"(c.srdV2), "s"(c.psf[1]), "i"(1024 * (q - 4)) : "memory");
  }
  if constexpr (M >= 1 && M <= 16 && KR) k_read<M - 1>(c, kslot_rd);
  if constexpr (M < 11 && !PEEL) rmax1<X, 3 * M>(c);
  if constexpr (M > BG_DEC) {
    constexpr int g = M - BG_DEC - 1, e_lo = (32 * g) / BG_N, e_hi = (32 * (g + 1)) / BG_N;
    exp1<X, 0, e_lo>(c);
    if constexpr (e_lo + 1 < e_hi && (M % 3 == 0 && M < 24 ? false : false)) exp1<X, 0, e_lo + 1>(c);
  }
  GAP();
  if constexpr (!PEEL) { pv_mfma<2 * M + 1, ZERO1>(c); GAP(); }
  if constexpr (M < 11 && !PEEL) { rmax1<X, 3 * M + 1>(c); rmax1<X, 3 * M + 2>(c); }
  if constexpr (M == BG_DEC) {
    if constexpr (PEEL) { newref<X, true, 0>(c); newref<X, true, 1>(c); newref<X, true, 2>(c); newref<X, true, 3>(c); }
    else {
      if (__builtin_expect(__any((int)(c.pmx0 > __float_as_int(THR))), 0)) {
        newref<X, false, 0>(c); newref<X, false, 1>(c); newref<X, false, 2>(c); newref<X, false, 3>(c);
        c.pend = true;
      }
    }
  }
  if constexpr (M > BG_DEC) {
    constexpr int g = M - BG_DEC - 1, e_lo = (32 * g) / BG_N, e_hi = (32 * (g + 1)) / BG_N;
    constexpr int p_lo = g ? (32 * (g - 1)) / BG_N : 0, p_hi = g ? (32 * g) / BG_N : 0;
    if constexpr (p_lo + 0 < p_hi) sum1<X, 0, p_lo>(c);
    if constexpr (p_lo + 1 < p_hi) sum1<X, 0, p_lo + 1>(c);
    if constexpr (e_lo + 1 < e_hi) exp1<X, 0, e_lo + 1>(c);
  }
  if constexpr (M == 31) asm volatile("" :: "v"(c.S[X][0][0]), "v"(c.S[X][0][1]), "v"(c.S[X][0][2]), "v"(c.S[X][0][3]), "v"(c.S[X][1][0]), "v"(c.S[X][1][1]), "v"(c.S[X][1][2]), "v"(c.S[X][1][3]),
                                         "v"(c.NEGM[0]), "v"(c.NEGM[1]), "v"(c.NEGM[2]), "v"(c.NEGM[3]));
  GAP();
}
template <int X, int... Es> DI void sums0(W4C& c, iseq<Es...>) { (sum1<X, 0, (32 * (BG_N - 1)) / BG_N + Es>(c), ...); }
template <int X, bool PEEL, bool ZERO1, int DK, int DV, bool KR, int... Ms> DI void phaseB_(W4C& c, const int t, iseq<Ms...>) {
  const unsigned kslot_rd = (unsigned)((t + 1) & 1) * 0x4000u;
  if constexpr (PEEL) asm volatile("s_nop 7" ::: "memory");
  if constexpr (!PEEL) asm volatile("s_nop 1" ::: "memory");
  GAP();
  (gapB<X, PEEL, ZERO1, DK, DV, KR, Ms>(c, kslot_rd), ...);
  GAP(); asm volatile("s_nop 1" ::: "memory"); GAP();
  sums0<X>(c, make_iseq<32 - (32 * (BG_N - 1)) / BG_N>{});
  if constexpr (!PEEL) {
    if (__builtin_expect(c.pend, 0)) {
      float t0, t1;
      asm volatile("s_nop 7\n\ts_nop 7\n\t"
        "v_accvgpr_read_b32 %0, a0\n\tv_mul_f32 %0, %2, %0\n\tv_accvgpr_write_b32 a0, %0\n\t"
        "v_accvgpr_read_b32 %1, a1\n\tv_mul_f32 %1, %2, %1\n\tv_accvgpr_write_b32 a1, %1\n\t"
        "v_accvgpr_read_b32 %0, a2\n\tv_mul_f32 %0, %2, %0\n\tv_accvgpr_write_b32 a2, %0\n\t"
        "v_accvgpr_read_b32 %1, a3\n\tv_mul_f32 %1, %2, %1\n\tv_accvgpr_write_b32 a3, %1\n\t"
        "v_accvgpr_read_b32 %0, a4\n\tv_mul_f32 %0, %3, %0\n\tv_accvgpr_write_b32 a4, %0\n\t"
        "v_accvgpr_read_b32 %1, a5\n\tv_mul_f32 %1, %3, %1\n\tv_accvgpr_write_b32 a5, %1\n\t"
        "v_accvgpr_read_b32 %0, a6\n\tv_mul_f32 %0, %3, %0\n\tv_accvgpr_write_b32 a6, %0\n\t"
        "v_accvgpr_read_b32 %1, a7\n\tv_mul_f32 %1, %3, %1\n\tv_accvgpr_write_b32 a7, %1\n\t"
        "v_accvgpr_read_b32 %0, a8\n\tv_mul_f32 %0, %4, %0\n\tv_accvgpr_write_b32 a8, %0\n\t"
        "v_accvgpr_read_b32 %1, a9\n\tv_mul_f32 %1, %4, %1\n\tv_accvgpr_write_b32 a9, %1\n\t"
        "v_accvgpr_read_b32 %0, a10\n\tv_mul_f32 %0, %4, %0\n\tv_accvgpr_write_b32 a10, %0\n\t"
        "v_accvgpr_read_b32 %1, a11\n\tv_mul_f32 %1, %4, %1\n\tv_accvgpr_write_b32 a11, %1\n\t"
        "v_accvgpr_read_b32 %0, a12\n\tv_mul_f32 %0, %5, %0\n\tv_accvgpr_write_b32 a12, %0\n\t"
        "v_accvgpr_read_b32 %1, a13\n\tv_mul_f32 %1, %5, %1\n\tv_accvgpr_write_b32 a13, %1\n\t"
        "v_accvgpr_read_b32 %0, a14\n\tv_mul_f32 %0, %5, %0\n\tv_accvgpr_write_b32 a14, %0\n\t"
        "v_accvgpr_read_b32 %1, a15\n\tv_mul_f32 %1, %5, %1\n\tv_accvgpr_write_b32 a15, %1\n\t"
        "v_accvgpr_read_b32 %0, a16\n\tv_mul_f32 %0, %2, %0\n\tv_accvgpr_write_b32 a16, %0\n\t"
        "v_accvgpr_read_b32 %1, a17\n\tv_mul_f32 %1, %2, %1\n\tv_accvgpr_write_b32 a17, %1\n\t"
        "v_accvgpr_read_b32 %0, a18\n\tv_mul_f32 %0, %2, %0\n\tv_accvgpr_write_b32 a18, %0\n\t"
        "v_accvgpr_read_b32 %1, a19\n\tv_mul_f32 %1, %2, %1\n\tv_accvgpr_write_b32 a19, %1\n\t"
        "v_accvgpr_read_b32 %0, a20\n\tv_mul_f32 %0, %3, %0\n\tv_accvgpr_write_b32 a20, %0\n\t"
        "v_accvgpr_read_b32 %1, a21\n\tv_mul_f32 %1, %3, %1\n\tv_accvgpr_write_b32 a21, %1\n\t"
        "v_accvgpr_read_b32 %0, a22\n\tv_mul_f32 %0, %3, %0\n\tv_accvgpr_write_b32 a22, %0\n\t"
        "v_accvgpr_read_b32 %1, a23\n\tv_mul_f32 %1, %3, %1\n\tv_accvgpr_write_b32 a23, %1\n\t"
        "v_accvgpr_read_b32 %0, a24\n\tv_mul_f32 %0, %4, %0\n\tv_accvgpr_write_b32 a24, %0\n\t"
        "v_accvgpr_read_b32 %1, a25\n\tv_mul_f32 %1, %4, %1\n\tv_accvgpr_write_b32 a25, %1\n\t"
        "v_accvgpr_read_b32 %0, a26\n\tv_mul_f32 %0, %4, %0\n\tv_accvgpr_write_b32 a26, %0\n\t"
        "v_accvgpr_read_b32 %1, a27\n\tv_mul_f32 %1, %4, %1\n\tv_accvgpr_write_b32 a27, %1\n\t"
        "v_accvgpr_read_b32 %0, a28\n\tv_mul_f32 %0, %5, %0\n\tv_accvgpr_write_b32 a28, %0\n\t"
        "v_accvgpr_read_b32 %1, a29\n\tv_mul_f32 %1, %5, %1\n\tv_accvgpr_write_b32 a29, %1\n\t"
        "v_accvgpr_read_b32 %0, a30\n\tv_mul_f32 %0, %5, %0\n\tv_accvgpr_write_b32 a30, %0\n\t"
        "v_accvgpr_read_b32 %1, a31\n\tv_mul_f32 %1, %5, %1\n\tv_accvgpr_write_b32 a31, %1\n\t"
        "v_accvgpr_read_b32 %0, a32\n\tv_mul_f32 %0, %2, %0\n\tv_accvgpr_write_b32 a32, %0\n\t"
        "v_accvgpr_read_b32 %1, a33\n\tv_mul_f32 %1, %2, %1\n\tv_accvgpr_write_b32 a33, %1\n\t"
        "v_accvgpr_read_b32 %0, a34\n\tv_mul_f32 %0, %2, %0\n\tv_accvgpr_write_b32 a34, %0\n\t"
        "v_accvgpr_read_b32 %1, a35\n\tv_mul_f32 %1, %2, %1\n\tv_accvgpr_write_b32 a35, %1\n\t"
        "v_accvgpr_read_b32 %0, a36\n\tv_mul_f32 %0, %3, %0\n\tv_accvgpr_write_b32 a36, %0\n\t"
        "v_accvgpr_read_b32 %1, a37\n\tv_mul_f32 %1, %3, %1\n\tv_accvgpr_write_b32 a37, %1\n\t"
        "v_accvgpr_read_b32 %0, a38\n\tv_mul_f32 %0, %3, %0\n\tv_accvgpr_write_b32 a38, %0\n\t"
        "v_accvgpr_read_b32 %1, a39\n\tv_mul_f32 %1, %3, %1\n\tv_accvgpr_write_b32 a39, %1\n\t"
        "v_accvgpr_read_b32 %0, a40\n\tv_mul_f32 %0, %4, %0\n\tv_accvgpr_write_b32 a40, %0\n\t"
        "v_accvgpr_read_b32 %1, a41\n\tv_mul_f32 %1, %4, %1\n\tv_accvgpr_write_b32 a41, %1\n\t"
        "v_accvgpr_read_b32 %0, a42\n\tv_mul_f32 %0, %4, %0\n\tv_accvgpr_write_b32 a42, %0\n\t"
        "v_accvgpr_read_b32 %1, a43\n\tv_mul_f32 %1, %4, %1\n\tv_accvgpr_write_b32 a43, %1\n\t"
        "v_accvgpr_read_b32 %0, a44\n\tv_mul_f32 %0, %5, %0\n\tv_accvgpr_write_b32 a44, %0\n\t"
        "v_accvgpr_read_b32 %1, a45\n\tv_mul_f32 %1, %5, %1\n\tv_accvgpr_write_b32 a45, %1\n\t"
        "v_accvgpr_read_b32 %0, a46\n\tv_mul_f32 %0, %5, %0\n\tv_accvgpr_write_b32 a46, %0\n\t"
        "v_accvgpr_read_b32 %1, a47\n\tv_mul_f32 %1, %5, %1\n\tv_accvgpr_write_b32 a47, %1\n\t"
        "v_accvgpr_read_b32 %0, a48\n\tv_mul_f32 %0, %2, %0\n\tv_accvgpr_write_b32 a48, %0\n\t"
        "v_accvgpr_read_b32 %1, a49\n\tv_mul_f32 %1, %2, %1\n\tv_accvgpr_write_b32 a49, %1\n\t"
        "v_accvgpr_read_b32 %0, a50\n\tv_mul_f32 %0, %2, %0\n\tv_accvgpr_write_b32 a50, %0\n\t"
        "v_accvgpr_read_b32 %1, a51\n\tv_mul_f32 %1, %2, %1\n\tv_accvgpr_write_b32 a51, %1\n\t"
        "v_accvgpr_read_b32 %0, a52\n\tv_mul_f32 %0, %3, %0\n\tv_accvgpr_write_b32 a52, %0\n\t"
        "v_accvgpr_read_b32 %1, a53\n\tv_mul_f32 %1, %3, %1\n\tv_accvgpr_write_b32 a53, %1\n\t"
        "v_accvgpr_read_b32 %0, a54\n\tv_mul_f32 %0, %3, %0\n\tv_accvgpr_write_b32 a54, %0\n\t"
        "v_accvgpr_read_b32 %1, a55\n\tv_mul_f32 %1, %3, %1\n\tv_accvgpr_write_b32 a55, %1\n\t"
        "v_accvgpr_read_b32 %0, a56\n\tv_mul_f32 %0, %4, %0\n\tv_accvgpr_write_b32 a56, %0\n\t"
        "v_accvgpr_read_b32 %1, a57\n\tv_mul_f32 %1, %4, %1\n\tv_accvgpr_write_b32 a57, %1\n\t"
        "v_accvgpr_read_b32 %0, a58\n\tv_mul_f32 %0, %4, %0\n\tv_accvgpr_write_b32 a58, %0\n\t"
        "v_accvgpr_read_b32 %1, a59\n\tv_mul_f32 %1, %4, %1\n\tv_accvgpr_write_b32 a59, %1\n\t"
        "v_accvgpr_read_b32 %0, a60\n\tv_mul_f32 %0, %5, %0\n\tv_accvgpr_write_b32 a60, %0\n\t"
        "v_accvgpr_read_b32 %1, a61\n\tv_mul_f32 %1, %5, %1\n\tv_accvgpr_write_b32 a61, %1\n\t"
        "v_accvgpr_read_b32 %0, a62\n\tv_mul_f32 %0, %5, %0\n\tv_accvgpr_write_b32 a62, %0\n\t"
        "v_accvgpr_read_b32 %1, a63\n\tv_mul_f32 %1, %5, %1\n\tv_accvgpr_write_b32 a63, %1\n\t"
        "v_accvgpr_read_b32 %0, a64\n\tv_mul_f32 %0, %2, %0\n\tv_accvgpr_write_b32 a64, %0\n\t"
        "v_accvgpr_read_b32 %1, a65\n\tv_mul_f32 %1, %2, %1\n\tv_accvgpr_write_b32 a65, %1\n\t"
        "v_accvgpr_read_b32 %0, a66\n\tv_mul_f32 %0, %2, %0\n\tv_accvgpr_write_b32 a66, %0\n\t"
        "v_accvgpr_read_b32 %1, a67\n\tv_mul_f32 %1, %2, %1\n\tv_accvgpr_write_b32 a67, %1\n\t"
        "v_accvgpr_read_b32 %0, a68\n\tv_mul_f32 %0, %3, %0\n\tv_accvgpr_write_b32 a68, %0\n\t"
        "v_accvgpr_read_b32 %1, a69\n\tv_mul_f32 %1, %3, %1\n\tv_accvgpr_write_b32 a69, %1\n\t"
        "v_accvgpr_read_b32 %0, a70\n\tv_mul_f32 %0, %3, %0\n\tv_accvgpr_write_b32 a70, %0\n\t"
        "v_accvgpr_read_b32 %1, a71\n\tv_mul_f32 %1, %3, %1\n\tv_accvgpr_write_b32 a71, %1\n\t"
        "v_accvgpr_read_b32 %0, a72\n\tv_mul_f32 %0, %4, %0\n\tv_accvgpr_write_b32 a72, %0\n\t"
        "v_accvgpr_read_b32 %1, a73\n\tv_mul_f32 %1, %4, %1\n\tv_accvgpr_write_b32 a73, %1\n\t"
        "v_accvgpr_read_b32 %0, a74\n\tv_mul_f32 %0, %4, %0\n\tv_accvgpr_write_b32 a74, %0\n\t"
        "v_accvgpr_read_b32 %1, a75\n\tv_mul_f32 %1, %4, %1\n\tv_accvgpr_write_b32 a75, %1\n\t"
        "v_accvgpr_read_b32 %0, a76\n\tv_mul_f32 %0, %5, %0\n\tv_accvgpr_write_b32 a76, %0\n\t"
        "v_accvgpr_read_b32 %1, a77\n\tv_mul_f32 %1, %5, %1\n\tv_accvgpr_write_b32 a77, %1\n\t"
        "v_accvgpr_read_b32 %0, a78\n\tv_mul_f32 %0, %5, %0\n\tv_accvgpr_write_b32 a78, %0\n\t"
        "v_accvgpr_read_b32 %1, a79\n\tv_mul_f32 %1, %5, %1\n\tv_accvgpr_write_b32 a79, %1\n\t"
        "v_accvgpr_read_b32 %0, a80\n\tv_mul_f32 %0, %2, %0\n\tv_accvgpr_write_b32 a80, %0\n\t"
        "v_accvgpr_read_b32 %1, a81\n\tv_mul_f32 %1, %2, %1\n\tv_accvgpr_write_b32 a81, %1\n\t"
        "v_accvgpr_read_b32 %0, a82\n\tv_mul_f32 %0, %2, %0\n\tv_accvgpr_write_b32 a82, %0\n\t"
        "v_accvgpr_read_b32 %1, a83\n\tv_mul_f32 %1, %2, %1\n\tv_accvgpr_write_b32 a83, %1\n\t"
        "v_accvgpr_read_b32 %0, a84\n\tv_mul_f32 %0, %3, %0\n\tv_accvgpr_write_b32 a84, %0\n\t"
        "v_accvgpr_read_b32 %1, a85\n\tv_mul_f32 %1, %3, %1\n\tv_accvgpr_write_b32 a85, %1\n\t"
        "v_accvgpr_read_b32 %0, a86\n\tv_mul_f32 %0, %3, %0\n\tv_accvgpr_write_b32 a86, %0\n\t"
        "v_accvgpr_read_b32 %1, a87\n\tv_mul_f32 %1, %3, %1\n\tv_accvgpr_write_b32 a87, %1\n\t"
        "v_accvgpr_read_b32 %0, a88\n\tv_mul_f32 %0, %4, %0\n\tv_accvgpr_write_b32 a88, %0\n\t"
        "v_accvgpr_read_b32 %1, a89\n\tv_mul_f32 %1, %4, %1\n\tv_accvgpr_write_b32 a89, %1\n\t"
        "v_accvgpr_read_b32 %0, a90\n\tv_mul_f32 %0, %4, %0\n\tv_accvgpr_write_b32 a90, %0\n\t"
        "v_accvgpr_read_b32 %1, a91\n\tv_mul_f32 %1, %4, %1\n\tv_accvgpr_write_b32 a91, %1\n\t"
        "v_accvgpr_read_b32 %0, a92\n\tv_mul_f32 %0, %5, %0\n\tv_accvgpr_write_b32 a92, %0\n\t"
        "v_accvgpr_read_b32 %1, a93\n\tv_mul_f32 %1, %5, %1\n\tv_accvgpr_write_b32 a93, %1\n\t"
        "v_accvgpr_read_b32 %0, a94\n\tv_mul_f32 %0, %5, %0\n\tv_accvgpr_write_b32 a94, %0\n\t"
        "v_accvgpr_read_b32 %1, a95\n\tv_mul_f32 %1, %5, %1\n\tv_accvgpr_write_b32 a95, %1\n\t"
        "v_accvgpr_read_b32 %0, a96\n\tv_mul_f32 %0, %2, %0\n\tv_accvgpr_write_b32 a96, %0\n\t"
        "v_accvgpr_read_b32 %1, a97\n\tv_mul_f32 %1, %2, %1\n\tv_accvgpr_write_b32 a97, %1\n\t"
        "v_accvgpr_read_b32 %0, a98\n\tv_mul_f32 %0, %2, %0\n\tv_accvgpr_write_b32 a98, %0\n\t"
        "v_accvgpr_read_b32 %1, a99\n\tv_mul_f32 %1, %2, %1\n\tv_accvgpr_write_b32 a99, %1\n\t"
        "v_accvgpr_read_b32 %0, a100\n\tv_mul_f32 %0, %3, %0\n\tv_accvgpr_write_b32 a100, %0\n\t"
        "v_accvgpr_read_b32 %1, a101\n\tv_mul_f32 %1, %3, %1\n\tv_accvgpr_write_b32 a101, %1\n\t"
        "v_accvgpr_read_b32 %0, a102\n\tv_mul_f32 %0, %3, %0\n\tv_accvgpr_write_b32 a102, %0\n\t"
        "v_accvgpr_read_b32 %1, a103\n\tv_mul_f32 %1, %3, %1\n\tv_accvgpr_write_b32 a103, %1\n\t"
        "v_accvgpr_read_b32 %0, a104\n\tv_mul_f32 %0, %4, %0\n\tv_accvgpr_write_b32 a104, %0\n\t"
        "v_accvgpr_read_b32 %1, a105\n\tv_mul_f32 %1, %4, %1\n\tv_accvgpr_write_b32 a105, %1\n\t"
        "v_accvgpr_read_b32 %0, a106\n\tv_mul_f32 %0, %4, %0\n\tv_accvgpr_write_b32 a106, %0\n\t"
        "v_accvgpr_read_b32 %1, a107\n\tv_mul_f32 %1, %4, %1\n\tv_accvgpr_write_b32 a107, %1\n\t"
        "v_accvgpr_read_b32 %0, a108\n\tv_mul_f32 %0, %5, %0\n\tv_accvgpr_write_b32 a108, %0\n\t"
        "v_accvgpr_read_b32 %1, a109\n\tv_mul_f32 %1, %5, %1\n\tv_accvgpr_write_b32 a109, %1\n\t"
        "v_accvgpr_read_b32 %0, a110\n\tv_mul_f32 %0, %5, %0\n\tv_accvgpr_write_b32 a110, %0\n\t"
        "v_accvgpr_read_b32 %1, a111\n\tv_mul_f32 %1, %5, %1\n\tv_accvgpr_write_b32 a111, %1\n\t"
        "v_accvgpr_read_b32 %0, a112\n\tv_mul_f32 %0, %2, %0\n\tv_accvgpr_write_b32 a112, %0\n\t"
        "v_accvgpr_read_b32 %1, a113\n\tv_mul_f32 %1, %2, %1\n\tv_accvgpr_write_b32 a113, %1\n\t"
        "v_accvgpr_read_b32 %0, a114\n\tv_mul_f32 %0, %2, %0\n\tv_accvgpr_write_b32 a114, %0\n\t"
        "v_accvgpr_read_b32 %1, a115\n\tv_mul_f32 %1, %2, %1\n\tv_accvgpr_write_b32 a115, %1\n\t"
        "v_accvgpr_read_b32 %0, a116\n\tv_mul_f32 %0, %3, %0\n\tv_accvgpr_write_b32 a116, %0\n\t"
        "v_accvgpr_read_b32 %1, a117\n\tv_mul_f32 %1, %3, %1\n\tv_accvgpr_write_b32 a117, %1\n\t"
        "v_accvgpr_read_b32 %0, a118\n\tv_mul_f32 %0, %3, %0\n\tv_accvgpr_write_b32 a118, %0\n\t"
        "v_accvgpr_read_b32 %1, a119\n\tv_mul_f32 %1, %3, %1\n\tv_accvgpr_write_b32 a119, %1\n\t"
        "v_accvgpr_read_b32 %0, a120\n\tv_mul_f32 %0, %4, %0\n\tv_accvgpr_write_b32 a120, %0\n\t"
        "v_accvgpr_read_b32 %1, a121\n\tv_mul_f32 %1, %4, %1\n\tv_accvgpr_write_b32 a121, %1\n\t"
        "v_accvgpr_read_b32 %0, a122\n\tv_mul_f32 %0, %4, %0\n\tv_accvgpr_write_b32 a122, %0\n\t"
        "v_accvgpr_read_b32 %1, a123\n\tv_mul_f32 %1, %4, %1\n\tv_accvgpr_write_b32 a123, %1\n\t"
        "v_accvgpr_read_b32 %0, a124\n\tv_mul_f32 %0, %5, %0\n\tv_accvgpr_write_b32 a124, %0\n\t"
        "v_accvgpr_read_b32 %1, a125\n\tv_mul_f32 %1, %5, %1\n\tv_accvgpr_write_b32 a125, %1\n\t"
        "v_accvgpr_read_b32 %0, a126\n\tv_mul_f32 %0, %5, %0\n\tv_accvgpr_write_b32 a126, %0\n\t"
        "v_accvgpr_read_b32 %1, a127\n\tv_mul_f32 %1, %5, %1\n\tv_accvgpr_write_b32 a127, %1\n\t"
        "s_nop 1" : "=&v"(t0), "=&v"(t1) : "v"(c.fct[0]), "v"(c.fct[1]), "v"(c.fct[2]), "v"(c.fct[3]) : W4_ACLOB);
      c.pend = false;
    }
  }
  asm volatile("s_waitcnt lgkmcnt(0)" ::: "memory");
  GAP();
}
template <int X, bool PEEL, bool ZERO1, int DK = 1, int DV = 1, bool KR = true> DI void phaseB(W4C& c, const int t) { phaseB_<X, PEEL, ZERO1, DK, DV, KR>(c, t, make_iseq<32>{}); }

template <int... Is> DI void q_reads(W4C& c, unsigned qoff, iseq<Is...>) { (q_read<Is>(c, qoff), ...); }
template <int... Is> DI void k_reads(W4C& c, unsigned slot, iseq<Is...>) { (k_read<Is>(c, slot), ...); }
template <int... Is> DI void tr_reads(W4C& c, unsigned vslot, iseq<Is...>) { (tr_read<Is>(c, vslot), ...); }
template <int X, int... Cs> DI void packs(W4C& c, iseq<Cs...>) { (pack1<X, (Cs >> 4), (Cs & 15)>(c), ...); }
template <int X, int... Es> DI void exps1(W4C& c, iseq<Es...>) { (exp1<X, 1, Es>(c), ...); }
template <int X, int... Es> DI void sums1(W4C& c, iseq<Es...>) { (sum1<X, 1, Es>(c), ...); }
template <bool Z, int... Ns> DI void pv_all(W4C& c, iseq<Ns...>) { (pv_mfma<Ns, Z>(c), ...); }
template <int R> DI f32x4 read_o4() {
  f32x4 o;
  asm volatile("v_accvgpr_read_b32 %0, a[%c4]\n\tv_accvgpr_read_b32 %1, a[%c5]\n\tv_accvgpr_read_b32 %2, a[%c6]\n\tv_accvgpr_read_b32 %3, a[%c7]"
               : "=v"(o[0]), "=v"(o[1]), "=v"(o[2]), "=v"(o[3]) : "i"(R), "i"(R + 1), "i"(R + 2), "i"(R + 3));
  return o;
}

typedef short s16x8 __attribute__((ext_vector_type(8)));
DI float bf2f(unsigned short u) { return __uint_as_float(((unsigned)u) << 16); }
DI unsigned short f2bf(float f) { return (unsigned short)(cvtpk(f, 0.f) & 0xffffu); }
DI float rbf(float f) { return __uint_as_float(cvtpk(f, 0.f) << 16); }

DI float dpp_sum8(float v) {
  v += __uint_as_float(__builtin_amdgcn_update_dpp(0u, __float_as_uint(v), 0xB1, 0xf, 0xf, false));
  v += __uint_as_float(__builtin_amdgcn_update_dpp(0u, __float_as_uint(v), 0x4E, 0xf, 0xf, false));
  v += __uint_as_float(__builtin_amdgcn_update_dpp(0u, __float_as_uint(v), 0x141, 0xf, 0xf, false));
  return v;
}
DI float dpp_sum16(float v) {
  v += __uint_as_float(__builtin_amdgcn_update_dpp(0u, __float_as_uint(v), 0xB1, 0xf, 0xf, false));
  v += __uint_as_float(__builtin_amdgcn_update_dpp(0u, __float_as_uint(v), 0x4E, 0xf, 0xf, false));
  v += __uint_as_float(__builtin_amdgcn_update_dpp(0u, __float_as_uint(v), 0x141, 0xf, 0xf, false));
  v += __uint_as_float(__builtin_amdgcn_update_dpp(0u, __float_as_uint(v), 0x140, 0xf, 0xf, false));
  return v;
}
template <bool MOD>
DI void ln_rows16(const unsigned short* __restrict__ x, const float* __restrict__ sh, const float* __restrict__ sc, unsigned short* __restrict__ out, const int row0) {
  const int t = threadIdx.x, li = t & 15, row = row0 + (t >> 4);
  u32x4 xv[4];
#pragma unroll
  for (int j = 0; j < 4; ++j) xv[j] = *reinterpret_cast<const u32x4*>(x + (long)row * DM + (li + 16 * j) * 8);
  f32x4 cv[8], sv[8];
  if constexpr (MOD) {
#pragma unroll
    for (int j = 0; j < 4; ++j) {
      cv[2 * j] = *reinterpret_cast<const f32x4*>(sc + (li + 16 * j) * 8); cv[2 * j + 1] = *reinterpret_cast<const f32x4*>(sc + (li + 16 * j) * 8 + 4);
      sv[2 * j] = *reinterpret_cast<const f32x4*>(sh + (li + 16 * j) * 8); sv[2 * j + 1] = *reinterpret_cast<const f32x4*>(sh + (li + 16 * j) * 8 + 4);
    }
  }
  float v[32]; float s = 0.f;
#pragma unroll
  for (int j = 0; j < 4; ++j)
#pragma unroll
    for (int e = 0; e < 4; ++e) { v[8 * j + 2 * e] = __uint_as_float(xv[j][e] << 16); v[8 * j + 2 * e + 1] = __uint_as_float(xv[j][e] & 0xffff0000u); s += v[8 * j + 2 * e] + v[8 * j + 2 * e + 1]; }
  const float mean = dpp_sum16(s) * (1.f / DM);
  float q = 0.f;
#pragma unroll
  for (int i = 0; i < 32; ++i) { const float d = v[i] - mean; q += d * d; }
  const float var = rbf(dpp_sum16(q) * (1.f / DM)), mb = rbf(mean);
  const float den = rbf(sqrtf(rbf(var + 1e-5f)));
  float r = __builtin_amdgcn_rcpf(den); r = r * (2.f - den * r);
  const int rb = row >> 5, rr = row & 31;
#pragma unroll
  for (int j = 0; j < 4; ++j) {
    float y[8];
#pragma unroll
    for (int e = 0; e < 8; ++e) {
      y[e] = rbf(v[8 * j + e] - mb) * r;
      if constexpr (MOD) y[e] = rbf(rbf(y[e]) * rbf(1.f + cv[2 * j + (e >> 2)][e & 3])) + sv[2 * j + (e >> 2)][e & 3];
    }
    u32x4 o4;
    if constexpr (MOD) o4 = u32x4{cvtpk(y[0], y[1]), cvtpk(y[2], y[3]), cvtpk(y[4], y[5]), cvtpk(y[6], y[7])};
    else {
#pragma unroll
      for (int e = 0; e < 4; ++e) o4[e] = __builtin_bit_cast(unsigned, __builtin_amdgcn_cvt_pkrtz(rbf(y[2 * e]), rbf(y[2 * e + 1])));
    }
    const int c = li + 16 * j;
    *reinterpret_cast<u32x4*>(out + ((long)(rb * 32 + (c >> 1)) * 64 + (c & 1) * 32 + rr) * 8) = o4;
  }
}

DI float silu_bf(unsigned short u) { const float v = bf2f(u); return rbf(v / (1.f + __expf(-v))); }
DI u32x4 ld16(const void* p) { u32x4 r; asm volatile("global_load_dwordx4 %0, %1, off" : "=v"(r) : "v"(p) : "memory"); return r; }
__global__ __launch_bounds__(512, 2) void k_prep(const unsigned short* __restrict__ c, const unsigned short* __restrict__ w, float* __restrict__ mod,
                                                 const unsigned short* __restrict__ x, unsigned short* __restrict__ xh, unsigned* __restrict__ cnt,
                                                 const unsigned short* __restrict__ wq) {
  __shared__ __attribute__((aligned(16))) float red[64][68];
  __shared__ __attribute__((aligned(16))) float sc[512];
  const int t = threadIdx.x;
  const unsigned blk = blockIdx.x >= 72u ? blockIdx.x - 72u : blockIdx.x + 112u;
  if (blk < 48) {
    unsigned cu;
    asm volatile("global_load_ushort %0, %1, off" : "=v"(cu) : "v"(c + t) : "memory");
    const int cb = blk * 64;
    const int cg = t & 7, kq = t >> 3;
    const unsigned short* wp = w + (long)(kq * 8) * 3072 + cb + cg * 8;
    u32x4 wv[8];
#pragma unroll
    for (int i = 0; i < 8; ++i) wv[i] = ld16(wp + (long)i * 3072);
    asm volatile("s_waitcnt vmcnt(8)" : "+v"(cu) :: "memory");
    sc[t] = silu_bf((unsigned short)cu);
    __syncthreads();
    asm volatile("s_waitcnt vmcnt(0)" : "+v"(wv[0]), "+v"(wv[1]), "+v"(wv[2]), "+v"(wv[3]), "+v"(wv[4]), "+v"(wv[5]), "+v"(wv[6]), "+v"(wv[7]) :: "memory");
    float acc[8] = {0.f, 0.f, 0.f, 0.f, 0.f, 0.f, 0.f, 0.f};
#pragma unroll
    for (int i = 0; i < 8; ++i) {
      const float s = sc[kq * 8 + i];
#pragma unroll
      for (int e = 0; e < 4; ++e) { acc[2 * e] += s * __uint_as_float(wv[i][e] << 16); acc[2 * e + 1] += s * __uint_as_float(wv[i][e] & 0xffff0000u); }
    }
#pragma unroll
    for (int j = 0; j < 8; ++j) red[kq][cg * 8 + j] = acc[j];
    __syncthreads();
    {
      const int col = t >> 3, part = t & 7;
      float s = 0.f;
#pragma unroll
      for (int i = 0; i < 8; ++i) s += red[part * 8 + i][col];
      s += __uint_as_float(__builtin_amdgcn_update_dpp(0u, __float_as_uint(s), 0xB1, 0xf, 0xf, false));
      s += __uint_as_float(__builtin_amdgcn_update_dpp(0u, __float_as_uint(s), 0x4E, 0xf, 0xf, false));
      s += __uint_as_float(__builtin_amdgcn_update_dpp(0u, __float_as_uint(s), 0x141, 0xf, 0xf, false));
      if (part == 0) mod[cb + col] = rbf(s);
    }
  } else if (blk >= 176) {
    unsigned long long pa, pt;
    asm volatile("s_getpc_b64 s[40:41]\n\ts_add_u32 s40, s40, k_attn@rel32@lo+4\n\ts_addc_u32 s41, s41, k_attn@rel32@hi+12\n\ts_mov_b64 %0, s[40:41]" : "=s"(pa) :: "scc", "s40", "s41");
    asm volatile("s_getpc_b64 s[40:41]\n\ts_add_u32 s40, s40, k_tail@rel32@lo+4\n\ts_addc_u32 s41, s41, k_tail@rel32@hi+12\n\ts_mov_b64 %0, s[40:41]" : "=s"(pt) :: "scc", "s40", "s41");
    const char* p = (const char*)pa + t * 16;
    const char* q = (const char*)pt + t * 16;
    u32x4 d0, d1, d2, d3;
    asm volatile("global_load_dwordx4 %0, %4, off\n\tglobal_load_dwordx4 %1, %5, off\n\tglobal_load_dwordx4 %2, %6, off\n\tglobal_load_dwordx4 %3, %7, off\n\t"
                 "global_load_dwordx4 %0, %8, off\n\tglobal_load_dwordx4 %1, %9, off\n\tglobal_load_dwordx4 %2, %10, off\n\tglobal_load_dwordx4 %3, %11, off\n\ts_waitcnt vmcnt(0)"
                 : "=&v"(d0), "=&v"(d1), "=&v"(d2), "=&v"(d3)
                 : "v"(p), "v"(p + 8192), "v"(p + 16384), "v"(p + 24576), "v"(p + 32768), "v"(p + 40960), "v"(q), "v"(q + 8192) : "memory");
  } else if (blk >= 112) {
    const unsigned short* p = wq + (size_t)(blk - 112) * 32768 + t * 8;
    u32x4 d0, d1, d2, d3;
    asm volatile("global_load_dwordx4 %0, %4, off\n\tglobal_load_dwordx4 %1, %5, off\n\tglobal_load_dwordx4 %2, %6, off\n\tglobal_load_dwordx4 %3, %7, off\n\t"
                 "global_load_dwordx4 %0, %8, off\n\tglobal_load_dwordx4 %1, %9, off\n\tglobal_load_dwordx4 %2, %10, off\n\tglobal_load_dwordx4 %3, %11, off\n\ts_waitcnt vmcnt(0)"
                 : "=&v"(d0), "=&v"(d1), "=&v"(d2), "=&v"(d3)
                 : "v"(p), "v"(p + 4096), "v"(p + 8192), "v"(p + 12288), "v"(p + 16384), "v"(p + 20480), "v"(p + 24576), "v"(p + 28672) : "memory");
  } else {
    if (blk == 48 && t < 48) cnt[32 * t] = 0u;
    ln_rows16<false>(x, nullptr, nullptr, xh, (blk - 48) * 32);
  }
}

#define FSLOT 0x10000u
#define CSOFF (VRING + 0x4000u)
constexpr unsigned WRS = 8192u;

struct QP {
  u32x4 xr[8][2];
  bf16x4 lo[3][4], up[3][4];
  bf16x8 pk[2][2];
  f32x4 c1[2][2], sa[2][2];
  unsigned vwb, vcs, vxo, vwsrc, m0f, sofw, xs0, xs1, m0p, sop;
  __amdgpu_buffer_rsrc_t srdP;
  __amdgpu_buffer_rsrc_t srdX, srdW;
};
constexpr int qp_issued_before_slot(int g) { return 24 + 2 * (g < 24 ? g : 24) + 2 * (g < 18 ? g : 18) + 2 * (g < 24 ? 0 : g < 28 ? g - 24 : 4); }
constexpr int qp_xidx(int g) { return g < 4 ? 4 + 2 * (g + 1) : g < 8 ? 16 + 2 * (g - 3) : qp_issued_before_slot(g - 8) + 2; }
constexpr int qp_widx(int t) { return t == 0 ? 4 : t == 1 ? 16 : qp_issued_before_slot(2 * t - 2); }
constexpr int qp_slot_of_tile(int t) { return t < 6 ? t : t - 6; }

template <int G> DI void qp_xload(QP& q) {
  asm volatile("buffer_load_dwordx4 %0, %1, %2, %3 offen" : "=v"(q.xr[G & 7][0]) : "v"(q.vxo), "s"(q.srdX), "s"(q.xs0 + 1024u * G) : "memory");
  asm volatile("buffer_load_dwordx4 %0, %1, %2, %3 offen" : "=v"(q.xr[G & 7][1]) : "v"(q.vxo), "s"(q.srdX), "s"(q.xs1 + 1024u * G) : "memory");
}
template <int T, int P> DI void qp_wdma1(QP& q) {
  asm volatile("s_mov_b32 m0, %0\n\ts_nop 0\n\tbuffer_load_dwordx4 %1, %2, %3 offen lds"
               :: "s"(q.m0f + 0x4000u * qp_slot_of_tile(T) + 1024u * P), "v"(q.vwsrc), "s"(q.srdW), "s"(q.sofw + WRS * (64u * T + 4u * (P >> 1)) + 128u * (P & 1)) : "m0", "memory");
}
template <int T> DI void qp_wdma(QP& q) { qp_wdma1<T, 0>(q); qp_wdma1<T, 1>(q); qp_wdma1<T, 2>(q); qp_wdma1<T, 3>(q); }
template <int TK, int P> DI void qp_kdma1(W4C& c) {
  asm volatile("s_mov_b32 m0, %0\n\ts_nop 0\n\tbuffer_load_dwordx4 %1, %2, %3 offen lds"
               :: "s"(c.m0k + (unsigned)(TK & 1) * 0x4000u + 1024u * P), "v"(c.vksrc[P]), "s"(c.srdK), "s"(KTILEB * (unsigned)TK + c.sofk + KRS * 4u * P) : "m0", "memory");
}
template <int TV, int P> DI void qp_vdma1(W4C& c) {
  asm volatile("s_mov_b32 m0, %0\n\ts_nop 0\n\tbuffer_load_dwordx4 %1, %2, %3 offen lds"
               :: "s"(c.m0v + (unsigned)(TV & 1) * 0x4000u + 1024u * P), "v"(c.vvsrc), "s"(c.srdV), "s"(KTILEB * (unsigned)TV + c.sofv + KRS * 8u * (P >> 1) + 128u * (P & 1)) : "m0", "memory");
}
template <int G, int J> DI void qp_extra(QP& q, W4C& c) {
  constexpr int P = 2 * (G & 1) + J;
  if constexpr (G < 12) qp_wdma1<2 + (G >> 1), P>(q);
  else if constexpr (G < 14) qp_kdma1<0, P>(c);
  else if constexpr (G < 16) qp_vdma1<0, P>(c);
  else if constexpr (G < 18) qp_kdma1<1, P>(c);
  else if constexpr (G >= 24 && G < 28)
    asm volatile("s_mov_b32 m0, %0\n\ts_nop 0\n\tbuffer_load_dwordx4 %1, %2, %3 offen lds" :: "s"(q.m0p), "v"(q.vxo), "s"(q.srdP), "s"(q.sop + 1024u * (2 * (G - 24) + J)) : "m0", "memory");
}
template <int G, int I> DI void qp_tr(QP& q, unsigned base) {
  constexpr int ks = G & 3, db = I >> 1;
  if constexpr ((I & 1) == 0) asm volatile("ds_read_b64_tr_b16 %0, %1 offset:%2" : "=v"(q.lo[G % 3][db]) : "v"(base), "i"(4096 * ks + 512 * db) : "memory");
  else                        asm volatile("ds_read_b64_tr_b16 %0, %1 offset:%2" : "=v"(q.up[G % 3][db]) : "v"(base), "i"(4096 * ks + 2048 + 512 * db) : "memory");
}
template <int G> DI void qp_cs(QP& q) {
  asm volatile("ds_read_b128 %0, %1 offset:%2" : "=v"(q.c1[G & 1][0]) : "v"(q.vcs), "i"(64 * G) : "memory");
  asm volatile("ds_read_b128 %0, %1 offset:%2" : "=v"(q.c1[G & 1][1]) : "v"(q.vcs), "i"(64 * G + 16) : "memory");
  asm volatile("ds_read_b128 %0, %1 offset:%2" : "=v"(q.sa[G & 1][0]) : "v"(q.vcs), "i"(64 * G + 2048) : "memory");
  asm volatile("ds_read_b128 %0, %1 offset:%2" : "=v"(q.sa[G & 1][1]) : "v"(q.vcs), "i"(64 * G + 2064) : "memory");
}
template <int G, int QB, int H> DI void qp_mod2(QP& q) {
  unsigned w0, w1; float t0, t1, t2, t3;
  asm("v_fma_mix_f32 %2, %6, %8, %12 op_sel_hi:[1,0,0]\n\tv_fma_mix_f32 %3, %6, %9, %13 op_sel:[1,0,0] op_sel_hi:[1,0,0]\n\t"
      "v_fma_mix_f32 %4, %7, %10, %14 op_sel_hi:[1,0,0]\n\tv_fma_mix_f32 %5, %7, %11, %15 op_sel:[1,0,0] op_sel_hi:[1,0,0]\n\t"
      "v_cvt_pk_bf16_f32 %0, %2, %3\n\tv_cvt_pk_bf16_f32 %1, %4, %5"
      : "=&v"(w0), "=&v"(w1), "=&v"(t0), "=&v"(t1), "=&v"(t2), "=&v"(t3)
      : "v"(q.xr[G & 7][QB][2 * H]), "v"(q.xr[G & 7][QB][2 * H + 1]),
        "v"(q.c1[G & 1][H][0]), "v"(q.c1[G & 1][H][1]), "v"(q.c1[G & 1][H][2]), "v"(q.c1[G & 1][H][3]),
        "v"(q.sa[G & 1][H][0]), "v"(q.sa[G & 1][H][1]), "v"(q.sa[G & 1][H][2]), "v"(q.sa[G & 1][H][3]));
  u32x4 w = __builtin_bit_cast(u32x4, q.pk[G & 1][QB]);
  w[2 * H] = w0; w[2 * H + 1] = w1;
  q.pk[G & 1][QB] = __builtin_bit_cast(bf16x8, w);
}
#define QP_CS(G) "+v"(q.c1[(G) & 1][0]), "+v"(q.c1[(G) & 1][1]), "+v"(q.sa[(G) & 1][0]), "+v"(q.sa[(G) & 1][1])
template <int G, int I> DI void qp_mfma(QP& q) {
  constexpr int db = I >> 1, qb = I & 1, ao = AO_(db, qb);
  const bf16x8 vf = __builtin_shufflevector(q.lo[G % 3][db], q.up[G % 3][db], 0, 1, 2, 3, 4, 5, 6, 7);
  if constexpr (G == 0) asm volatile("s_nop 1\n\tv_mfma_f32_32x32x16_bf16 a[%c2:%c3], %0, %1, 0" :: "v"(vf), "v"(q.pk[G & 1][qb]), "i"(ao), "i"(ao + 15) : W4_ACLOB);
  else                  asm volatile("v_mfma_f32_32x32x16_bf16 a[%c2:%c3], %0, %1, a[%c2:%c3]" :: "v"(vf), "v"(q.pk[G & 1][qb]), "i"(ao), "i"(ao + 15) : W4_ACLOB);
}
#define QP_LOHI(G) "+v"(q.lo[(G) % 3][0]), "+v"(q.lo[(G) % 3][1]), "+v"(q.lo[(G) % 3][2]), "+v"(q.lo[(G) % 3][3]), "+v"(q.up[(G) % 3][0]), "+v"(q.up[(G) % 3][1]), "+v"(q.up[(G) % 3][2]), "+v"(q.up[(G) % 3][3])

template <int G> DI void qp_slot(QP& q, W4C& c) {
  constexpr int T = (G + 2) >> 2;
  qp_mfma<G, 0>(q); GAP();
  if constexpr (G + 2 < 32 && ((G + 2) & 3) == 0) {
    constexpr int nv = qp_issued_before_slot(G) - qp_widx(T);
    asm volatile("s_waitcnt vmcnt(%c8) lgkmcnt(0)\n\ts_barrier" : QP_LOHI(G + 1) : "i"(nv < 63 ? nv : 63) : "memory");
  }
  const unsigned base = q.vwb + 0x4000u * qp_slot_of_tile(T);
  if constexpr (G + 2 < 32) { qp_tr<G + 2, 0>(q, base); qp_tr<G + 2, 1>(q, base); qp_tr<G + 2, 2>(q, base); qp_tr<G + 2, 3>(q, base); }
  qp_mfma<G, 1>(q); GAP();
  if constexpr (G + 1 < 32) { qp_mod2<G + 1, 0, 0>(q); GAP(); }
  if constexpr (G + 8 < 32) asm volatile("buffer_load_dwordx4 %0, %1, %2, %3 offen" : "=v"(q.xr[G & 7][0]) : "v"(q.vxo), "s"(q.srdX), "s"(q.xs0 + 1024u * (G + 8)) : "memory");
  qp_mfma<G, 2>(q); GAP();
  if constexpr (G + 2 < 32) { qp_tr<G + 2, 4>(q, base); qp_tr<G + 2, 5>(q, base); qp_tr<G + 2, 6>(q, base); qp_tr<G + 2, 7>(q, base); }
  qp_mfma<G, 3>(q); GAP();
  if constexpr (G + 1 < 32) { qp_mod2<G + 1, 0, 1>(q); GAP(); }
  if constexpr (G + 8 < 32) asm volatile("buffer_load_dwordx4 %0, %1, %2, %3 offen" : "=v"(q.xr[G & 7][1]) : "v"(q.vxo), "s"(q.srdX), "s"(q.xs1 + 1024u * (G + 8)) : "memory");
  qp_mfma<G, 4>(q); GAP();
  if constexpr (G + 2 < 32) qp_cs<G + 2>(q);
  qp_mfma<G, 5>(q); GAP();
  if constexpr (G + 1 < 32) { qp_mod2<G + 1, 1, 0>(q); GAP(); }
  qp_extra<G, 0>(q, c);
  qp_mfma<G, 6>(q); GAP();
  if constexpr (G + 1 < 32) { qp_mod2<G + 1, 1, 1>(q); GAP(); }
  qp_mfma<G, 7>(q); GAP();
  qp_extra<G, 1>(q, c);
  if constexpr (G + 2 < 32) {
    constexpr int nv = qp_issued_before_slot(G + 1) - qp_xidx(G + 2);
    asm volatile("s_waitcnt vmcnt(%c14) lgkmcnt(0)" : QP_LOHI(G + 2), QP_CS(G + 2), "+v"(q.xr[(G + 2) & 7][0]), "+v"(q.xr[(G + 2) & 7][1]) : "i"(nv < 63 ? nv : 63) : "memory");
  }
  GAP();
}
template <int... Gs> DI void qp_all(QP& q, W4C& c, iseq<Gs...>) { (qp_slot<Gs>(q, c), ...); }

extern "C" __global__ __launch_bounds__(256, 1)
void k_attn(const unsigned short* __restrict__ xh, const unsigned short* __restrict__ wq, const float* __restrict__ mod,
            const unsigned short* __restrict__ Kb, const unsigned short* __restrict__ Vb, unsigned short* __restrict__ O,
            const unsigned short* __restrict__ wo, const unsigned short* __restrict__ wg, const unsigned short* __restrict__ wu, const unsigned short* __restrict__ wd) {
  extern __shared__ __attribute__((aligned(16))) char lds[];
  const unsigned tid = threadIdx.x, lane = tid & 63u, wid = tid >> 6, l32 = lane & 31u, hi = lane >> 5;
  constexpr int T = NT;
  const float C = 0.12751743082f;
  W4C c;
  c.T = T; c.THR = THR;
  c.ldsb = (unsigned)(size_t)lds;
  {
    const unsigned l16 = lane & 15u, g = lane >> 4;
#pragma unroll
    for (int j = 0; j < 4; ++j) c.vkb[j] = c.ldsb + KRING + 256u * l16 + 16u * ((4u * j + g) ^ l16);
#pragma unroll
    for (int par = 0; par < 2; ++par) c.vvb[par] = c.ldsb + VRING + 2048u * (g >> 1) + 64u * (4u * (g & 1u) + ((lane >> 2) & 3u)) + 32u * ((unsigned)par ^ (g & 1u)) + 8u * (lane & 3u);
#pragma unroll
    for (int q = 0; q < 4; ++q) c.vksrc[q] = KRS * g + 16u * (l16 ^ (4u * q + g));
    c.vvsrc = KRS * ((lane >> 2) & 7u) + 64u * hi + 16u * ((lane & 3u) ^ (2u * ((lane >> 4) & 1u)));
    c.vvsrc2[0] = c.vvsrc + 896u; c.vvsrc2[1] = c.vvsrc;
  }
  c.m0k = rfl(c.ldsb + KRING + 4096u * wid); c.m0v = rfl(c.ldsb + VRING + 4096u * wid);
  c.sofk = rfl(KRS * 16u * wid); c.sofv = rfl(KRS * 16u * wid);

  const unsigned bid = blockIdx.x;
  const unsigned h = (bid & 7u) * 4u + (bid >> 6), y = (bid >> 3) & 7u;
  const unsigned row0 = 256u * y + 64u * rfl(wid);
  c.srdK = __builtin_amdgcn_make_buffer_rsrc((void*)((const char*)Kb + (size_t)h * SEQ * 256), (short)0, SEQ * 256, SRD_WORD3);
  c.srdV = __builtin_amdgcn_make_buffer_rsrc((void*)((const char*)Vb + (size_t)h * SEQ * 256), (short)0, SEQ * 256, SRD_WORD3);
  c.srdV2 = __builtin_amdgcn_make_buffer_rsrc((void*)((const char*)Vb + (size_t)h * SEQ * 256 - 896), (short)0, SEQ * 256 + 896, SRD_WORD3);
  {
    QP q;
    q.srdX = __builtin_amdgcn_make_buffer_rsrc((void*)xh, (short)0, SEQ * DM * 2, SRD_WORD3);
    q.srdW = __builtin_amdgcn_make_buffer_rsrc((void*)((const char*)wq + h * 256u), (short)0, (int)(DM * WRS - h * 256u), SRD_WORD3);
    q.vxo = lane * 16u;
    q.xs0 = (row0 >> 5) * 32768u; q.xs1 = q.xs0 + 32768u;
    q.vwsrc = WRS * (8u * ((lane >> 4) & 1u) + ((lane >> 2) & 3u)) + 64u * hi + 16u * (lane & 3u);
    q.m0f = rfl(c.ldsb + FSLOT + 4096u * wid);
    q.sofw = rfl(WRS * 16u * wid);
    q.vwb = c.ldsb + FSLOT + ((((lane & 1u) << 1 | ((lane >> 1) & 1u)) << 3) | (((lane >> 2) & 3u) << 6) | (((lane >> 4) & 1u) << 5) | (hi << 8));
    q.vcs = c.ldsb + CSOFF + 32u * hi;
    {
      const unsigned short* pb = bid < 128u ? wo : bid < 171u ? wg : bid < 214u ? wu : wd;
      const unsigned pi = bid < 128u ? bid : bid < 171u ? bid - 128u : bid < 214u ? bid - 171u : bid - 214u;
      q.srdP = __builtin_amdgcn_make_buffer_rsrc((void*)pb, (short)0, bid < 128u ? 4096 * 1024 : 1376 * 1024, SRD_WORD3);
      q.sop = pi * 32768u + rfl(wid) * 8192u;
      q.m0p = rfl(c.ldsb + CSOFF + 8192u + 1024u * wid);
    }
    f32x2 sa2, ca2;
    {
      __amdgpu_buffer_rsrc_t srdM = __builtin_amdgcn_make_buffer_rsrc((void*)mod, (short)0, 3072 * 4, SRD_WORD3);
      const unsigned mo = tid * 8u;
      asm volatile("s_nop 4\n\tbuffer_load_dwordx2 %0, %2, %3, 0 offen\n\tbuffer_load_dwordx2 %1, %2, %3, 0 offen offset:2048" : "=&v"(sa2), "=&v"(ca2) : "v"(mo), "s"(srdM) : "memory");
    }
    qp_wdma<0>(q); qp_xload<0>(q); qp_xload<1>(q); qp_xload<2>(q); qp_xload<3>(q);
    {
      asm volatile("s_waitcnt vmcnt(12)" : "+v"(sa2), "+v"(ca2) :: "memory");
      f32x2 c12 = {rbf(1.f + ca2[0]), rbf(1.f + ca2[1])};
      *reinterpret_cast<f32x2*>(lds + CSOFF + 8 * tid) = c12;
      *reinterpret_cast<f32x2*>(lds + CSOFF + 2048 + 8 * tid) = sa2;
    }
    asm volatile("s_waitcnt vmcnt(4) lgkmcnt(0)\n\ts_barrier" : "+v"(q.xr[0][0]), "+v"(q.xr[0][1]), "+v"(q.xr[1][0]), "+v"(q.xr[1][1]) :: "memory");
    {
      const unsigned base = q.vwb;
      qp_cs<0>(q); qp_cs<1>(q);
      qp_tr<0, 0>(q, base); qp_tr<0, 1>(q, base); qp_tr<0, 2>(q, base); qp_tr<0, 3>(q, base); qp_tr<0, 4>(q, base); qp_tr<0, 5>(q, base); qp_tr<0, 6>(q, base); qp_tr<0, 7>(q, base);
      qp_tr<1, 0>(q, base); qp_tr<1, 1>(q, base); qp_tr<1, 2>(q, base); qp_tr<1, 3>(q, base); qp_tr<1, 4>(q, base); qp_tr<1, 5>(q, base); qp_tr<1, 6>(q, base); qp_tr<1, 7>(q, base);
      qp_wdma<1>(q); qp_xload<4>(q); qp_xload<5>(q); qp_xload<6>(q); qp_xload<7>(q);
      asm volatile("s_waitcnt lgkmcnt(0)" : QP_LOHI(0), QP_LOHI(1), QP_CS(0), QP_CS(1) :: "memory");
      GAP();
      qp_mod2<0, 0, 0>(q); qp_mod2<0, 0, 1>(q); qp_mod2<0, 1, 0>(q); qp_mod2<0, 1, 1>(q);
      GAP();
    }
    qp_all(q, c, make_iseq<32>{});
    asm volatile("s_waitcnt vmcnt(8)\n\ts_barrier" ::: "memory");
    k_reads(c, 0u, make_iseq<16>{});
    asm volatile("s_nop 7\n\ts_nop 7" ::: "memory");
    {
      char* stg = lds + FSLOT + 16384u * wid;
#pragma unroll
      for (int qb = 0; qb < 2; ++qb)
#pragma unroll
        for (int db = 0; db < 4; ++db) {
          float o[16];
          W4_READ_O(db, qb, o);
#pragma unroll
          for (int s = 0; s < 2; ++s) {
            u32x4 w;
#pragma unroll
            for (int e = 0; e < 4; ++e) w[e] = cvtpk(fmul_s(o[8 * s + 2 * e], C), fmul_s(o[8 * s + 2 * e + 1], C));
            *(u32x4*)(stg + 256u * (32u * qb + l32) + 16u * ((4u * db + 2u * s + hi) ^ (l32 & 15u))) = w;
          }
        }
      asm volatile("s_waitcnt lgkmcnt(0)" ::: "memory");
      q_reads(c, FSLOT + 16384u * wid, make_iseq<16>{});
    }
    asm volatile("s_waitcnt lgkmcnt(0)\n\ts_nop 1" ::: "memory");
  }

#pragma unroll
  for (int q = 0; q < 4; ++q) { c.mhat[q] = 0.f; c.lsum[q] = 0.f; c.fct[q] = 1.f; }
  c.pend = false;

  phaseA<0, true>(c, 0);  phaseB<0, true,  false>(c, 0);
  phaseA<1, false>(c, 1); phaseB<1, false, true>(c, 1);
  for (int t = 2; t < T - 2; t += 2) {
    phaseA<0, false>(c, t);     phaseB<0, false, false>(c, t);
    phaseA<1, false>(c, t + 1); phaseB<1, false, false>(c, t + 1);
  }
  phaseA<0, false>(c, T - 2); phaseB<0, false, false,  0,  1,  true>(c, T - 2);
  phaseA<1, false>(c, T - 1); phaseB<1, false, false,  0,  0,  false>(c, T - 1);
  {
    const unsigned vslot = (unsigned)((T - 1) & 1) * 0x4000u;
    tr_reads(c, vslot, make_iseq<32>{});
    exps1<1>(c, make_iseq<32>{});
    GAP(); asm volatile("s_nop 1" ::: "memory"); GAP();
    packs<1>(c, make_iseq<32>{});
    sums1<1>(c, make_iseq<32>{});
#pragma unroll
    for (int qb = 0; qb < 4; ++qb) {
      float rs = fadd_s(c.rs0[qb], c.rs1[qb]);
      { auto pr = __builtin_amdgcn_permlane16_swap(__float_as_uint(rs), __float_as_uint(rs), false, false); rs = fadd_s(__uint_as_float(pr[0]), __uint_as_float(pr[1])); }
      { auto pr = __builtin_amdgcn_permlane32_swap(__float_as_uint(rs), __float_as_uint(rs), false, false); rs = fadd_s(__uint_as_float(pr[0]), __uint_as_float(pr[1])); }
      c.lsum[qb] = rs;
    }
    asm volatile("s_waitcnt lgkmcnt(0)" : W4_VLO :: "memory"); asm volatile("" : W4_VUP :: "memory");
    GAP();
    v_concat(c);
    asm volatile("s_nop 1" ::: "memory");
    pv_all<false>(c, make_iseq<64>{});
    asm volatile("s_nop 7\n\ts_nop 7" ::: "memory");
  }
  {
    unsigned lb;
    asm volatile("v_mbcnt_lo_u32_b32 %0, -1, 0\n\tv_mbcnt_hi_u32_b32 %0, -1, %0" : "=v"(lb));
    auto epi = [&](auto QB) {
      constexpr int qb = decltype(QB)::value;
      const float il = c.lsum[qb] > 0.f ? __builtin_amdgcn_rcpf(c.lsum[qb]) : 0.f;
      char* ob = (char*)O + ((size_t)((row0 >> 5) + (qb >> 1)) * 256u + 8u * h + (lb >> 5)) * 1024u + (16u * (qb & 1) + (lb & 15u) + 32u * ((lb >> 4) & 1u)) * 16u;
      auto pair = [&](auto DP) {
        constexpr int db = 2 * decltype(DP)::value;
        const f32x4 oa = read_o4<BO_(db, qb)>(), ob4 = read_o4<BO_(db + 1, qb)>();
        const unsigned a0 = cvtpk(fmul_s(oa[0], il), fmul_s(oa[1], il)), a1 = cvtpk(fmul_s(oa[2], il), fmul_s(oa[3], il));
        const unsigned b0 = cvtpk(fmul_s(ob4[0], il), fmul_s(ob4[1], il)), b1 = cvtpk(fmul_s(ob4[2], il), fmul_s(ob4[3], il));
        auto r0 = __builtin_amdgcn_permlane32_swap(a0, b0, false, false);
        auto r1 = __builtin_amdgcn_permlane32_swap(a1, b1, false, false);
        u32x4 w = {r0[0], r1[0], r0[1], r1[1]};
        char* op = ob + db * 1024u;
        asm volatile("global_store_dwordx4 %0, %1, off sc1\n\ts_nop 1" :: "v"(op), "v"(w) : "memory");
      };
      pair(std::integral_constant<int, 0>{}); pair(std::integral_constant<int, 1>{}); pair(std::integral_constant<int, 2>{}); pair(std::integral_constant<int, 3>{});
    };
    epi(std::integral_constant<int, 0>{}); epi(std::integral_constant<int, 1>{}); epi(std::integral_constant<int, 2>{}); epi(std::integral_constant<int, 3>{});
  }
}

typedef __attribute__((address_space(1))) unsigned gu32;
DI void grp_arrive(unsigned* cnt) {
  asm volatile("s_waitcnt vmcnt(0)" ::: "memory");
  __syncthreads();
  if (threadIdx.x == 0) __hip_atomic_fetch_add((gu32*)cnt, 1u, __ATOMIC_RELAXED, __HIP_MEMORY_SCOPE_AGENT);
}
DI void grp_wait(unsigned* cnt) {
  if (threadIdx.x == 0) {
    for (unsigned spins = 0; spins < (1u << 18); ++spins) {
      if (__hip_atomic_load((gu32*)cnt, __ATOMIC_RELAXED, __HIP_MEMORY_SCOPE_AGENT) >= 16u) break;
      __builtin_amdgcn_s_sleep(1);
    }
  }
  asm volatile("s_barrier" ::: "memory");
}

struct G64 {
  u32x4 af[8][2];
  bf16x4 lo[2][2], up[2][2];
  f32x16 acc[2][2];
  unsigned vao, vwsrc, vtr, m0r, sa0, sa1, sw;
  __amdgpu_buffer_rsrc_t srdA, srdW;
};
constexpr int G64_P = 6;
template <int I, int NSTEP, int KS> DI unsigned g64_s(const unsigned s0) {
  unsigned s = s0 + I;
  if constexpr (NSTEP * 7 + I >= KS) s = s < (unsigned)KS ? s : (unsigned)(KS - 1);
  return s;
}
template <int I, int NSTEP, int KS> DI void g64_fetchW(G64& g, const unsigned s0) {
  const unsigned s = g64_s<I, NSTEP, KS>(s0);
  const unsigned sw = g.sw + s * 16384u, m0 = g.m0r + 2048u * (I & 7);
  asm volatile("s_mov_b32 m0, %0\n\ts_nop 0\n\tbuffer_load_dwordx4 %1, %2, %3 offen lds" :: "s"(m0), "v"(g.vwsrc), "s"(g.srdW), "s"(sw) : "m0", "memory");
  asm volatile("s_mov_b32 m0, %0\n\ts_nop 0\n\tbuffer_load_dwordx4 %1, %2, %3 offen lds" :: "s"(m0 + 1024u), "v"(g.vwsrc), "s"(g.srdW), "s"(sw + 8192u) : "m0", "memory");
}
template <int I, int NSTEP, int KS, bool PRE> DI void g64_fetchA(G64& g, const unsigned s0) {
  const unsigned s = g64_s<I, NSTEP, KS>(s0);
  if constexpr (PRE) {
    asm volatile("buffer_load_dwordx4 %0, %1, %2, %3 offen sc1" : "=v"(g.af[I & 7][0]) : "v"(g.vao), "s"(g.srdA), "s"(g.sa0 + s * 1024u) : "memory");
    asm volatile("buffer_load_dwordx4 %0, %1, %2, %3 offen sc1" : "=v"(g.af[I & 7][1]) : "v"(g.vao), "s"(g.srdA), "s"(g.sa1 + s * 1024u) : "memory");
  } else {
    asm volatile("buffer_load_dwordx4 %0, %1, %2, %3 offen" : "=v"(g.af[I & 7][0]) : "v"(g.vao), "s"(g.srdA), "s"(g.sa0 + s * 1024u) : "memory");
    asm volatile("buffer_load_dwordx4 %0, %1, %2, %3 offen" : "=v"(g.af[I & 7][1]) : "v"(g.vao), "s"(g.srdA), "s"(g.sa1 + s * 1024u) : "memory");
  }
}
template <int I> DI void g64_tr(G64& g) {
  constexpr int o = 2048 * (I & 7);
  asm volatile("ds_read_b64_tr_b16 %0, %1 offset:%2" : "=v"(g.lo[I & 1][0]) : "v"(g.vtr), "i"(o) : "memory");
  asm volatile("ds_read_b64_tr_b16 %0, %1 offset:%2" : "=v"(g.up[I & 1][0]) : "v"(g.vtr), "i"(o + 1024) : "memory");
  asm volatile("ds_read_b64_tr_b16 %0, %1 offset:%2" : "=v"(g.lo[I & 1][1]) : "v"(g.vtr), "i"(o + 512) : "memory");
  asm volatile("ds_read_b64_tr_b16 %0, %1 offset:%2" : "=v"(g.up[I & 1][1]) : "v"(g.vtr), "i"(o + 1536) : "memory");
}
constexpr int g64_younger(int I, int J, int NSTEP, bool PRE) {
  const int nf = (I + G64_P < NSTEP ? I + G64_P : NSTEP);
  const int issued = 4 * nf;
  const int need = (PRE && J < G64_P) ? 2 * G64_P + 2 * (J + 1) : 4 * (J + 1);
  return issued - need;
}
template <int I, int NSTEP, int KS, bool PRE> DI void g64_step(G64& g, const unsigned s0) {
  if constexpr (I + 1 < NSTEP) {
    asm volatile("s_waitcnt vmcnt(%c6) lgkmcnt(0)" : "+v"(g.lo[I & 1][0]), "+v"(g.up[I & 1][0]), "+v"(g.lo[I & 1][1]), "+v"(g.up[I & 1][1]), "+v"(g.af[(I + 1) & 7][0]), "+v"(g.af[(I + 1) & 7][1])
                 : "i"(g64_younger(I, I + 1, NSTEP, PRE)) : "memory");
    g64_tr<I + 1>(g);
    if constexpr (I + G64_P < NSTEP) { g64_fetchW<I + G64_P, NSTEP, KS>(g, s0); g64_fetchA<I + G64_P, NSTEP, KS, PRE>(g, s0); }
  } else {
    asm volatile("s_waitcnt lgkmcnt(0)" : "+v"(g.lo[I & 1][0]), "+v"(g.up[I & 1][0]), "+v"(g.lo[I & 1][1]), "+v"(g.up[I & 1][1]) :: "memory");
  }
  u32x4 a0 = g.af[I & 7][0], a1 = g.af[I & 7][1];
  if constexpr (NSTEP * 7 + I >= KS) { if (s0 + I >= (unsigned)KS) { a0 = u32x4{0, 0, 0, 0}; a1 = a0; } }
  const bf16x8 w0 = __builtin_shufflevector(g.lo[I & 1][0], g.up[I & 1][0], 0, 1, 2, 3, 4, 5, 6, 7);
  const bf16x8 w1 = __builtin_shufflevector(g.lo[I & 1][1], g.up[I & 1][1], 0, 1, 2, 3, 4, 5, 6, 7);
  g.acc[0][0] = __builtin_amdgcn_mfma_f32_32x32x16_bf16(w0, __builtin_bit_cast(bf16x8, a0), g.acc[0][0], 0, 0, 0);
  g.acc[0][1] = __builtin_amdgcn_mfma_f32_32x32x16_bf16(w0, __builtin_bit_cast(bf16x8, a1), g.acc[0][1], 0, 0, 0);
  g.acc[1][0] = __builtin_amdgcn_mfma_f32_32x32x16_bf16(w1, __builtin_bit_cast(bf16x8, a0), g.acc[1][0], 0, 0, 0);
  g.acc[1][1] = __builtin_amdgcn_mfma_f32_32x32x16_bf16(w1, __builtin_bit_cast(bf16x8, a1), g.acc[1][1], 0, 0, 0);
  GAP();
}
template <int NSTEP, int KS, int... Is> DI void g64_proWA(G64& g, const unsigned s0, iseq<Is...>) { ((g64_fetchW<Is, NSTEP, KS>(g, s0), g64_fetchA<Is, NSTEP, KS, false>(g, s0)), ...); }
template <int NSTEP, int KS, int... Is> DI void g64_proW(G64& g, const unsigned s0, iseq<Is...>) { (g64_fetchW<Is, NSTEP, KS>(g, s0), ...); }
template <int NSTEP, int KS, int... Is> DI void g64_proA(G64& g, const unsigned s0, iseq<Is...>) { (g64_fetchA<Is, NSTEP, KS, true>(g, s0), ...); }
template <int NSTEP, int KS, bool PRE, int... Is> DI void g64_all(G64& g, const unsigned s0, iseq<Is...>) { (g64_step<Is, NSTEP, KS, PRE>(g, s0), ...); }
template <int KS> DI void g64_setup(G64& g, const unsigned short* Af, const unsigned short* W, const unsigned ldsb, const unsigned lane, const unsigned wid, const unsigned tm, const unsigned tn) {
  const unsigned hi = lane >> 5;
  g.srdA = __builtin_amdgcn_make_buffer_rsrc((void*)Af, (short)0, 64 * KS * 1024, SRD_WORD3);
  g.srdW = __builtin_amdgcn_make_buffer_rsrc((void*)W, (short)0, KS * 16 * 1024, SRD_WORD3);
  g.vao = lane * 16u;
  g.vwsrc = 1024u * ((lane >> 2) & 7u) + 64u * hi + 16u * (lane & 3u);
  g.vtr = ldsb + 16384u * wid + (((lane & 3u) << 3) | (((lane >> 2) & 3u) << 6) | (((lane >> 4) & 1u) << 5) | (hi << 8));
  g.m0r = ldsb + 16384u * wid;
  g.sa0 = (2u * tm) * (unsigned)KS * 1024u; g.sa1 = g.sa0 + (unsigned)KS * 1024u;
  g.sw = 128u * tn;
  g.acc[0][0] = f32x16{}; g.acc[0][1] = f32x16{}; g.acc[1][0] = f32x16{}; g.acc[1][1] = f32x16{};
}
DI u32x4 g64_finish(G64& g, char* lds, const unsigned tid, const unsigned lane, const unsigned wid, const u32x4 rv, const f32x4 g0, const f32x4 g1) {
  const unsigned hi = lane >> 5, l32 = lane & 31u, em = tid >> 3, en = (tid & 7u) * 8u;
  char* pw = lds + 16384u * wid;
#pragma unroll
  for (int nb = 0; nb < 2; ++nb)
#pragma unroll
    for (int mb = 0; mb < 2; ++mb)
#pragma unroll
      for (int q = 0; q < 4; ++q) {
        const unsigned m = 32u * mb + l32, ch = 8u * nb + 2u * q + hi;
        f32x4 v = {g.acc[nb][mb][4 * q], g.acc[nb][mb][4 * q + 1], g.acc[nb][mb][4 * q + 2], g.acc[nb][mb][4 * q + 3]};
        *(f32x4*)(pw + 256u * m + 16u * (ch ^ (m & 15u))) = v;
      }
  __syncthreads();
  f32x4 s0v = {0.f, 0.f, 0.f, 0.f}, s1v = s0v;
  const unsigned c0 = (en >> 2) ^ (em & 15u), c1 = ((en >> 2) + 1u) ^ (em & 15u);
#pragma unroll
  for (int w = 0; w < 8; ++w) {
    s0v += *(const f32x4*)(lds + 16384u * w + 256u * em + 16u * c0);
    s1v += *(const f32x4*)(lds + 16384u * w + 256u * em + 16u * c1);
  }
  u32x4 o4;
#pragma unroll
  for (int e = 0; e < 4; ++e) {
    const float ya = rbf(e < 2 ? s0v[2 * e] : s1v[2 * e - 4]), yb = rbf(e < 2 ? s0v[2 * e + 1] : s1v[2 * e - 3]);
    const float ga = e < 2 ? g0[2 * e] : g1[2 * e - 4], gb = e < 2 ? g0[2 * e + 1] : g1[2 * e - 3];
    const float ra = __uint_as_float(rv[e] << 16), rb = __uint_as_float(rv[e] & 0xffff0000u);
    o4[e] = cvtpk(ra + rbf(ga * ya), rb + rbf(gb * yb));
  }
  return o4;
}

struct GU {
  u32x4 hf[16];
  u32x4 hm[2];
  f32x4 c1[2], sb[2];
  float r, nmr;
  f32x16 acc[2][3];
  bf16x4 lo[2][6], up[2][6];
  unsigned vwsrc[3], vtr, m0r, swc, rowb, vho, sho, vcs;
  __amdgpu_buffer_rsrc_t srdG, srdU, srdH;
};
template <int I, int P> DI void gu_dma2(GU& g) {
  const unsigned m0 = g.m0r + 24576u * (I % 3);
  const unsigned rowo = g.rowb + 64u * I * (unsigned)(DF * 2) + g.swc;
  asm volatile("s_mov_b32 m0, %0\n\ts_nop 0\n\tbuffer_load_dwordx4 %1, %2, %3 offen lds" :: "s"(m0 + 1024u * P), "v"(g.vwsrc[P]), "s"(g.srdG), "s"(rowo) : "m0", "memory");
  asm volatile("s_mov_b32 m0, %0\n\ts_nop 0\n\tbuffer_load_dwordx4 %1, %2, %3 offen lds" :: "s"(m0 + 1024u * (3 + P)), "v"(g.vwsrc[P]), "s"(g.srdU), "s"(rowo) : "m0", "memory");
}
template <int I> DI void gu_dma(GU& g) { gu_dma2<I, 0>(g); gu_dma2<I, 1>(g); gu_dma2<I, 2>(g); }
template <int S> DI void gu_hload(GU& g) {
  asm volatile("buffer_load_dwordx4 %0, %1, %2, %3 offen sc1" : "=v"(g.hf[S]) : "v"(g.vho), "s"(g.srdH), "s"(g.sho + 1024u * S) : "memory");
}
template <int... Ss> DI void gu_hloads(GU& g, iseq<Ss...>) { (gu_hload<Ss>(g), ...); }
template <int S, int I> DI void gu_tr1(GU& g, const unsigned base) {
  constexpr int o = 1024 * (6 * (S & 3) + 3 * (I / 3)) + 64 * (I % 3);
  asm volatile("ds_read_b64_tr_b16 %0, %1 offset:%2" : "=v"(g.lo[S & 1][I]) : "v"(base), "i"(o) : "memory");
  asm volatile("ds_read_b64_tr_b16 %0, %1 offset:%2" : "=v"(g.up[S & 1][I]) : "v"(base), "i"(o + 8 * 192) : "memory");
}
template <int S> DI void gu_load(GU& g) {
  constexpr int T = S >> 2;
  if constexpr ((S & 3) == 0) {
    constexpr int nv = T == 0 ? 12 : T == 1 ? 8 : T == 2 ? 10 : 0;
    asm volatile("s_waitcnt vmcnt(%c4) lgkmcnt(0)\n\ts_barrier" : "+v"(g.hf[4 * T]), "+v"(g.hf[4 * T + 1]), "+v"(g.hf[4 * T + 2]), "+v"(g.hf[4 * T + 3]) : "i"(nv) : "memory");
    if constexpr (T == 1) gu_dma<3>(g);
  }
  asm volatile("ds_read_b128 %0, %1 offset:%2" : "=v"(g.c1[0]) : "v"(g.vcs), "i"(64 * S) : "memory");
  asm volatile("ds_read_b128 %0, %1 offset:%2" : "=v"(g.c1[1]) : "v"(g.vcs), "i"(64 * S + 16) : "memory");
  asm volatile("ds_read_b128 %0, %1 offset:%2" : "=v"(g.sb[0]) : "v"(g.vcs), "i"(64 * S + 2048) : "memory");
  asm volatile("ds_read_b128 %0, %1 offset:%2" : "=v"(g.sb[1]) : "v"(g.vcs), "i"(64 * S + 2064) : "memory");
  const unsigned base = g.vtr + 24576u * (T % 3);
  gu_tr1<S, 0>(g, base); gu_tr1<S, 1>(g, base); gu_tr1<S, 2>(g, base); gu_tr1<S, 3>(g, base); gu_tr1<S, 4>(g, base); gu_tr1<S, 5>(g, base);
}
template <int S> DI void gu_mod(GU& g) {
  u32x4 w;
#pragma unroll
  for (int e = 0; e < 4; ++e) {
    const float a = __builtin_fmaf(__uint_as_float(g.hf[S][e] << 16), g.r, g.nmr), b = __builtin_fmaf(__uint_as_float(g.hf[S][e] & 0xffff0000u), g.r, g.nmr);
    w[e] = cvtpk(__builtin_fmaf(a, g.c1[e >> 1][(2 * e) & 3], g.sb[e >> 1][(2 * e) & 3]), __builtin_fmaf(b, g.c1[e >> 1][(2 * e + 1) & 3], g.sb[e >> 1][(2 * e + 1) & 3]));
  }
  g.hm[S & 1] = w;
}
#define GU_W(X) "+v"(g.lo[X][0]), "+v"(g.lo[X][1]), "+v"(g.lo[X][2]), "+v"(g.lo[X][3]), "+v"(g.lo[X][4]), "+v"(g.lo[X][5]), "+v"(g.up[X][0]), "+v"(g.up[X][1]), "+v"(g.up[X][2]), "+v"(g.up[X][3]), "+v"(g.up[X][4]), "+v"(g.up[X][5])
template <int S, int I> DI void gu_mfma(GU& g) {
  const bf16x8 w = __builtin_shufflevector(g.lo[S & 1][I], g.up[S & 1][I], 0, 1, 2, 3, 4, 5, 6, 7);
  g.acc[I / 3][I % 3] = __builtin_amdgcn_mfma_f32_32x32x16_bf16(w, __builtin_bit_cast(bf16x8, g.hm[S & 1]), g.acc[I / 3][I % 3], 0, 0, 0);
}
template <int S> DI void gu_step(GU& g) {
  if constexpr (S + 1 < 16) gu_load<S + 1>(g);
  gu_mfma<S, 0>(g); gu_mfma<S, 1>(g); gu_mfma<S, 2>(g);
  GAP();
  if constexpr (S + 1 < 16) {
    asm volatile("s_waitcnt lgkmcnt(12)" : "+v"(g.c1[0]), "+v"(g.c1[1]), "+v"(g.sb[0]), "+v"(g.sb[1]) :: "memory");
    gu_mod<S + 1>(g);
  }
  gu_mfma<S, 3>(g); gu_mfma<S, 4>(g); gu_mfma<S, 5>(g);
  if constexpr (S + 1 < 16) asm volatile("s_waitcnt lgkmcnt(0)" : GU_W((S + 1) & 1) :: "memory");
  GAP();
}
template <int... Ss> DI void gu_all(GU& g, iseq<Ss...>) { (gu_step<Ss>(g), ...); }
DI float gelu_t(float x) {
  const float y = x * __builtin_fmaf(x * x, 0.044715f * -2.302208198f, -2.302208198f);
  return x * __builtin_amdgcn_rcpf(1.f + __builtin_amdgcn_exp2f(y));
}
constexpr int AUX_SC1 = 16;

extern "C" __global__ __launch_bounds__(512, 2) void k_tail(const unsigned short* __restrict__ Of, const unsigned short* __restrict__ wo, const unsigned short* __restrict__ x,
                                                 const float* __restrict__ mod, const unsigned short* __restrict__ wg, const unsigned short* __restrict__ wu,
                                                 const unsigned short* __restrict__ wd, unsigned short* x1, unsigned short* h2, unsigned short* af,
                                                 unsigned* cnt, float* stats, unsigned short* __restrict__ out) {
  extern __shared__ __attribute__((aligned(16))) char lds[];
  const unsigned tid = threadIdx.x, lane = tid & 63u, wid = rfl(tid >> 6), hi = lane >> 5;
  const unsigned bid = blockIdx.x, grp = 2u * (bid & 7u) + (bid >> 7), j = (bid >> 3) & 15u;
  const unsigned tm = 2u * grp + (j >> 3), tn = j & 7u;
  const unsigned ldsb = (unsigned)(size_t)lds;
  const unsigned em = tid >> 3, en = (tid & 7u) * 8u;
  unsigned* cA = cnt + 32u * grp; unsigned* cB = cnt + 32u * (16u + grp); unsigned* cC = cnt + 32u * (32u + grp);
  u32x4 x1v;
  {
    G64 g;
    g64_setup<256>(g, Of, wo, ldsb, lane, wid, tm, tn);
    const unsigned s0 = wid * 32u;
    u32x4 rv; f32x4 g0, g1;
    {
      __amdgpu_buffer_rsrc_t srdR = __builtin_amdgcn_make_buffer_rsrc((void*)x, (short)0, SEQ * DM * 2, SRD_WORD3);
      __amdgpu_buffer_rsrc_t srdG = __builtin_amdgcn_make_buffer_rsrc((void*)(mod + 1024), (short)0, DM * 4, SRD_WORD3);
      const unsigned ro = ((64u * tm + em) * 512u + 64u * tn + en) * 2u, go = (64u * tn + en) * 4u;
      asm volatile("s_nop 4\n\tbuffer_load_dwordx4 %0, %3, %5, 0 offen\n\tbuffer_load_dwordx4 %1, %4, %6, 0 offen\n\tbuffer_load_dwordx4 %2, %4, %6, 0 offen offset:16"
                   : "=&v"(rv), "=&v"(g0), "=&v"(g1) : "v"(ro), "v"(go), "s"(srdR), "s"(srdG) : "memory");
    }
    g64_proWA<32, 256>(g, s0, make_iseq<G64_P>{});
    asm volatile("s_waitcnt vmcnt(%c5)" : "+v"(g.af[0][0]), "+v"(g.af[0][1]), "+v"(rv), "+v"(g0), "+v"(g1) : "i"(4 * (G64_P - 1)) : "memory");
    g64_tr<0>(g);
    g64_all<32, 256, false>(g, s0, make_iseq<32>{});
    x1v = g64_finish(g, lds, tid, lane, wid, rv, g0, g1);
    const unsigned row = 64u * tm + em, ch = 8u * tn + (tid & 7u);
    __amdgpu_buffer_rsrc_t srdX1 = __builtin_amdgcn_make_buffer_rsrc((void*)x1, (short)0, SEQ * DM * 2, SRD_WORD3);
    __builtin_amdgcn_raw_buffer_store_b128(x1v, srdX1, (((row >> 5) * 32u + (ch >> 1)) * 64u + (ch & 1u) * 32u + (row & 31u)) * 16u, 0, AUX_SC1);
    float v[8], sm = 0.f;
#pragma unroll
    for (int e = 0; e < 4; ++e) { v[2 * e] = __uint_as_float(x1v[e] << 16); v[2 * e + 1] = __uint_as_float(x1v[e] & 0xffff0000u); sm += v[2 * e] + v[2 * e + 1]; }
    sm = dpp_sum8(sm);
    const float mt = sm * (1.f / 64.f);
    float m2 = 0.f;
#pragma unroll
    for (int e = 0; e < 8; ++e) { const float d = v[e] - mt; m2 += d * d; }
    m2 = dpp_sum8(m2);
    if ((tid & 7u) == 0u) {
      __amdgpu_buffer_rsrc_t srdS = __builtin_amdgcn_make_buffer_rsrc((void*)stats, (short)0, SEQ * 64, SRD_WORD3);
      u32x2 o2 = {__float_as_uint(sm), __float_as_uint(m2)};
      __builtin_amdgcn_raw_buffer_store_b64(o2, srdS, row * 64u + tn * 8u, 0, AUX_SC1);
    }
  }
  grp_arrive(cA);
  const unsigned rg = wid & 3u, kh = wid >> 2;
  const unsigned nb0 = j < 11u ? 3u * j : 33u + 2u * (j - 11u), nbc = j < 11u ? 3u : 2u;
  const unsigned rb = 4u * grp + rg;
  {
    GU g;
    g.srdG = __builtin_amdgcn_make_buffer_rsrc((void*)wg, (short)0, DM * DF * 2, SRD_WORD3);
    g.srdU = __builtin_amdgcn_make_buffer_rsrc((void*)wu, (short)0, DM * DF * 2, SRD_WORD3);
#pragma unroll
    for (int p = 0; p < 3; ++p) {
      const unsigned ci = 64u * p + lane, kap = ci / 12u, cc = ci - 12u * kap;
      g.vwsrc[p] = (unsigned)(DF * 2) * (8u * ((kap >> 2) & 1u) + 4u * (kap >> 3) + (kap & 3u)) + 16u * cc;
    }
    g.vtr = ldsb + 73728u * kh + 192u * (((lane >> 2) & 3u) + 4u * hi) + 32u * ((lane >> 4) & 1u) + 8u * (lane & 3u);
    g.m0r = ldsb + 73728u * kh + 6144u * rg;
    g.rowb = (256u * kh + 16u * rg) * (unsigned)(DF * 2);
    g.swc = 64u * nb0;
#pragma unroll
    for (int i = 0; i < 6; ++i) g.acc[i / 3][i % 3] = f32x16{};
    g.srdH = __builtin_amdgcn_make_buffer_rsrc((void*)x1, (short)0, SEQ * DM * 2, SRD_WORD3);
    g.sho = rb * 32768u + kh * 16384u; g.vho = lane * 16u;
    g.vcs = ldsb + 147456u + 1024u * kh + 32u * hi;
    {
      const float cbv = mod[2048 + tid], sbv = mod[1536 + tid];
      *(float*)(lds + 147456u + 4u * tid) = rbf(1.f + cbv);
      *(float*)(lds + 147456u + 2048u + 4u * tid) = sbv;
    }
    asm volatile("s_nop 4" ::: "memory");
    gu_dma<0>(g); gu_dma<1>(g); gu_dma<2>(g);
    grp_wait(cA);
    {
      f32x4 st[4];
      __amdgpu_buffer_rsrc_t srdS = __builtin_amdgcn_make_buffer_rsrc((void*)stats, (short)0, SEQ * 64, SRD_WORD3);
      const unsigned so = (rb * 32u + (lane & 31u)) * 64u;
      asm volatile("buffer_load_dwordx4 %0, %4, %5, 0 offen sc1\n\tbuffer_load_dwordx4 %1, %4, %5, 0 offen offset:16 sc1\n\t"
                   "buffer_load_dwordx4 %2, %4, %5, 0 offen offset:32 sc1\n\tbuffer_load_dwordx4 %3, %4, %5, 0 offen offset:48 sc1"
                   : "=&v"(st[0]), "=&v"(st[1]), "=&v"(st[2]), "=&v"(st[3]) : "v"(so), "s"(srdS) : "memory");
      gu_hloads(g, make_iseq<16>{});
      asm volatile("s_waitcnt vmcnt(16)" : "+v"(st[0]), "+v"(st[1]), "+v"(st[2]), "+v"(st[3]) :: "memory");
      float sm = 0.f;
#pragma unroll
      for (int i = 0; i < 4; ++i) sm += st[i][0] + st[i][2];
      const float mean = sm * (1.f / DM);
      float m2 = 0.f;
#pragma unroll
      for (int i = 0; i < 4; ++i) { const float d0 = st[i][0] * (1.f / 64.f) - mean, d1 = st[i][2] * (1.f / 64.f) - mean; m2 += st[i][1] + st[i][3] + 64.f * (d0 * d0 + d1 * d1); }
      const float var = rbf(m2 * (1.f / DM)), mb = rbf(mean);
      const float den = rbf(sqrtf(rbf(var + 1e-5f)));
      float r = __builtin_amdgcn_rcpf(den); r = r * (2.f - den * r);
      g.r = r; g.nmr = -mb * r;
    }
    gu_load<0>(g);
    asm volatile("s_waitcnt lgkmcnt(0)" : GU_W(0), "+v"(g.c1[0]), "+v"(g.c1[1]), "+v"(g.sb[0]), "+v"(g.sb[1]) :: "memory");
    gu_mod<0>(g);
    gu_all(g, make_iseq<16>{});
    __syncthreads();
    __amdgpu_buffer_rsrc_t srdA5 = __builtin_amdgcn_make_buffer_rsrc((void*)af, (short)0, 64 * 86 * 1024, SRD_WORD3);
    auto fin = [&](auto KH) {
      constexpr int kk = decltype(KH)::value;
      char* pw = lds + 12288u * wid + lane * 16u;
#pragma unroll
      for (int i = 0; i < 6; ++i)
#pragma unroll
        for (int q = 0; q < 2; ++q) {
          f32x4 v;
#pragma unroll
          for (int e = 0; e < 4; ++e) v[e] = g.acc[i / 3][i % 3][8 * (1 - kk) + 4 * q + e];
          *(f32x4*)(pw + 1024u * (2 * i + q)) = v;
        }
      __syncthreads();
      const char* pr = lds + 12288u * (wid ^ 4u) + lane * 16u;
      const unsigned ob = (rb * 86u + 2u * nb0 + kk) * 1024u + lane * 16u;
#pragma unroll
      for (int nb = 0; nb < 3; ++nb) {
        if ((unsigned)nb < nbc) {
          float a[8];
#pragma unroll
          for (int q = 0; q < 2; ++q) {
            const f32x4 pg = *(const f32x4*)(pr + 1024u * (2 * nb + q)), pu = *(const f32x4*)(pr + 1024u * (2 * (3 + nb) + q));
#pragma unroll
            for (int e = 0; e < 4; ++e)
              a[4 * q + e] = rbf(gelu_t(rbf(pg[e] + g.acc[0][nb][8 * kk + 4 * q + e]))) * rbf(pu[e] + g.acc[1][nb][8 * kk + 4 * q + e]);
          }
          const unsigned a0 = cvtpk(a[0], a[1]), a1 = cvtpk(a[2], a[3]), b0 = cvtpk(a[4], a[5]), b1 = cvtpk(a[6], a[7]);
          u32x4 w = {a0, a1, b0, b1};
          __builtin_amdgcn_raw_buffer_store_b128(w, srdA5, ob + 2u * nb * 1024u, 0, AUX_SC1);
        }
      }
    };
    if (kh) fin(std::integral_constant<int, 1>{}); else fin(std::integral_constant<int, 0>{});
  }
  grp_arrive(cC);
  {
    G64 g;
    g64_setup<86>(g, af, wd, ldsb, lane, wid, tm, tn);
    const unsigned s0 = wid * 11u;
    f32x4 g0, g1;
    {
      __amdgpu_buffer_rsrc_t srdG = __builtin_amdgcn_make_buffer_rsrc((void*)(mod + 2560), (short)0, DM * 4, SRD_WORD3);
      const unsigned go = (64u * tn + en) * 4u;
      asm volatile("s_nop 4\n\tbuffer_load_dwordx4 %0, %2, %3, 0 offen\n\tbuffer_load_dwordx4 %1, %2, %3, 0 offen offset:16" : "=&v"(g0), "=&v"(g1) : "v"(go), "s"(srdG) : "memory");
    }
    g64_proW<11, 86>(g, s0, make_iseq<G64_P>{});
    grp_wait(cC);
    g64_proA<11, 86>(g, s0, make_iseq<G64_P>{});
    asm volatile("s_waitcnt vmcnt(%c4)" : "+v"(g.af[0][0]), "+v"(g.af[0][1]), "+v"(g0), "+v"(g1) : "i"(2 * (G64_P - 1)) : "memory");
    g64_tr<0>(g);
    g64_all<11, 86, true>(g, s0, make_iseq<11>{});
    const u32x4 o4 = g64_finish(g, lds, tid, lane, wid, x1v, g0, g1);
    *(u32x4*)(out + ((size_t)(64u * tm + em) * 512u + 64u * tn + en)) = o4;
  }
}

extern "C" void kernel_launch(void* const* d_in, const int* in_sizes, int n_in, void* d_out, int out_size, void* d_ws, size_t ws_size, hipStream_t stream) {
  const unsigned short* x = (const unsigned short*)d_in[0];
  const unsigned short* kc = (const unsigned short*)d_in[1];
  const unsigned short* vc = (const unsigned short*)d_in[2];
  const unsigned short* wq = (const unsigned short*)d_in[3];
  const unsigned short* wo = (const unsigned short*)d_in[4];
  const unsigned short* wg = (const unsigned short*)d_in[6];
  const unsigned short* wu = (const unsigned short*)d_in[7];
  const unsigned short* wd = (const unsigned short*)d_in[8];
  const unsigned short* c = (const unsigned short*)d_in[9];
  const unsigned short* wm = (const unsigned short*)d_in[10];
  char* ws = (char*)d_ws;
  float* mod = (float*)ws;
  unsigned* cnt = (unsigned*)(ws + 65536);
  unsigned short* h2 = (unsigned short*)(ws + (1 << 20));
  unsigned short* xh = (unsigned short*)(ws + (4 << 20));
  unsigned short* o  = (unsigned short*)(ws + (20 << 20));
  unsigned short* x1 = (unsigned short*)(ws + (36 << 20));
  unsigned short* a  = (unsigned short*)(ws + (40 << 20));
  (void)hipFuncSetAttribute((const void*)k_attn, hipFuncAttributeMaxDynamicSharedMemorySize, (int)SHM_ATTN);
  float* stats = (float*)(ws + (512 << 10));
  (void)hipFuncSetAttribute((const void*)k_tail, hipFuncAttributeMaxDynamicSharedMemorySize, 151552);
  k_prep<<<48 + 64 + 72, 512, 0, stream>>>(c, wm, mod, x, xh, cnt, wq);
  k_attn<<<256, 256, SHM_ATTN, stream>>>(xh, wq, mod, kc, vc, o, wo, wg, wu, wd);
  k_tail<<<256, 512, 151552, stream>>>(o, wo, x, mod, wg, wu, wd, x1, h2, a, cnt, stats, (unsigned short*)d_out);
}
```
